```python
import torch

def _softmax(x, axis=-1):
    return torch.softmax(x, dim=axis)
def _gelu(x):
    return torch.nn.functional.gelu(x, approximate="tanh")
def _silu(x):
    return torch.nn.functional.silu(x)
def _sigmoid(x):
    return torch.sigmoid(x)
def _layernorm(x, axis=-1, eps=1e-5):
    m = x.mean(dim=axis, keepdim=True)
    v = x.var(dim=axis, keepdim=True, unbiased=False)
    return (x - m) / torch.sqrt(v + eps)
def _scatter_add(tgt, idx, src, axis=0):
    return tgt.clone().index_add_(axis, idx, src)
def _segment_sum(src, seg, n):
    out = torch.zeros((n,) + src.shape[1:], dtype=src.dtype)
    return out.index_add_(0, seg, src)
def _pad(x, pw):
    flat = tuple(v for pair in reversed(pw) for v in pair)
    return torch.nn.functional.pad(x, flat)

kernel_name = 'interleave_layers__ssm_conv_h3__swiglu_ffn__prompt'

def setup_inputs():

    _w = (lambda a: (lambda t: t.to(torch.bfloat16) if t.is_floating_point() else t)(torch.as_tensor(a)))
    def _A_setup():
        B, S, D = 1, 2048, 512
        ks = 4
        return {
            "q": _w((lambda *s: torch.randn(*s, dtype=torch.bfloat16) * 0.05)(B, S, D)),
            "k": _w((lambda *s: torch.randn(*s, dtype=torch.bfloat16) * 0.8)(B, S, D)),
            "v": _w((lambda *s: torch.randn(*s, dtype=torch.bfloat16) * 0.8)(B, S, D)),
            "shift_k": _w((lambda *s: torch.randn(*s, dtype=torch.bfloat16) * 0.8)(D, ks) * 0.5),
            "ssm_k": _w((lambda *s: torch.randn(*s, dtype=torch.bfloat16) * 0.8)(D, S) * (1.0 / S ** 0.5)),
            "ssm_D": _w((lambda *s: torch.randn(*s, dtype=torch.bfloat16) * 0.8)(D)),
        }
    _A_in = _A_setup()

    def _B_setup():
        B, S, D, F = 1, 2048, 512, 1376
        return {
            "x": _w((lambda *s: torch.randn(*s, dtype=torch.bfloat16) * 0.05)(B, S, D)),
            "w_gate": _w((lambda *s: torch.randn(*s, dtype=torch.bfloat16) * 0.1)(D, F)),
            "w_up": _w((lambda *s: torch.randn(*s, dtype=torch.bfloat16) * 0.1)(D, F)),
            "w_down": _w((lambda *s: torch.randn(*s, dtype=torch.bfloat16) * 0.1)(F, D)),
        }
    _B_in = _B_setup()

    return {
        "A_q": _A_in["q"],
        "A_k": _A_in["k"],
        "A_v": _A_in["v"],
        "A_shift_k": _A_in["shift_k"],
        "A_ssm_k": _A_in["ssm_k"],
        "A_ssm_D": _A_in["ssm_D"],
        "B_x": _B_in["x"],
        "B_w_gate": _B_in["w_gate"],
        "B_w_up": _B_in["w_up"],
        "B_w_down": _B_in["w_down"],
    }

def reference(A_q, A_k, A_v, A_shift_k, A_ssm_k, A_ssm_D, B_x, B_w_gate, B_w_up, B_w_down):
    def _A_ref(q, k, v, shift_k, ssm_k, ssm_D):
        B, S, D = q.shape
        ks = shift_k.shape[-1]
        k_pad = _pad(k, ((0, 0), (ks - 1, 0), (0, 0)))
        k_shift = torch.zeros_like(k)
        for t in range(ks):
            k_shift = k_shift + k_pad[:, t:t + S, :] * shift_k[:, ks - 1 - t][None, None, :]
        kv = k_shift * v
        L = 2 * S
        kv_pad = _pad(kv, ((0, 0), (0, S), (0, 0)))
        kf_pad = _pad(ssm_k, ((0, 0), (0, S)))
        kv_f = (lambda x, axes=(-2,-1): torch.fft.rfft2(x.float(), dim=axes))(kv_pad[:, None, :, :], axes=(1, 2))
        kf_f = (lambda x, axes=(-2,-1): torch.fft.rfft2(x.float(), dim=axes))(kf_pad[:, None, :], axes=(1, 2))
        kf_f = (lambda x, *d: x.permute(*d))(kf_f, 1, 2, 0)
        y_f = kv_f * kf_f[None, :, :, :]
        y = (lambda x, s, axes=(-2,-1): torch.fft.irfft2(x, s=s, dim=axes).to(torch.bfloat16))(y_f, (1, L), axes=(1, 2))[:, 0, :S, :]
        y = y + kv * ssm_D[None, None, :]
        return q * y

    def _B_ref(x, w_gate, w_up, w_down):
        g = _silu(x @ w_gate)
        u = x @ w_up
        return (g * u) @ w_down

    x = A_q
    x = x + _A_ref(_layernorm(x, axis=-1), A_k, A_v, A_shift_k, A_ssm_k, A_ssm_D)
    x = x + _B_ref(_layernorm(x, axis=-1), B_w_gate, B_w_up, B_w_down)
    x = x + _A_ref(_layernorm(x, axis=-1), A_k, A_v, A_shift_k, A_ssm_k, A_ssm_D)
    x = x + _B_ref(_layernorm(x, axis=-1), B_w_gate, B_w_up, B_w_down)
    return x
```

```cpp
#include <hip/hip_runtime.h>
#include <hip/hip_bf16.h>
#include <stdint.h>

typedef __attribute__((ext_vector_type(8))) __bf16   bf16x8;
typedef __attribute__((ext_vector_type(2))) __bf16   bf16x2;
typedef __attribute__((ext_vector_type(4))) float    f32x4;
typedef __attribute__((ext_vector_type(2))) float    f32x2;
typedef __attribute__((ext_vector_type(4))) unsigned u32x4;
typedef __attribute__((ext_vector_type(2))) unsigned u32x2;
typedef unsigned short u16;

constexpr int S_ = 2048, D_ = 512, F_ = 1376;
constexpr int NFT = 86;
constexpr int KS1 = 16;
constexpr int KS2 = 43;

__device__ __forceinline__ float bflo(unsigned u) { return __builtin_bit_cast(float, u << 16); }
__device__ __forceinline__ float bfhi(unsigned u) { return __builtin_bit_cast(float, u & 0xffff0000u); }
__device__ __forceinline__ unsigned pk2(float lo, float hi) {
    f32x2 v = {lo, hi};
    bf16x2 r = __builtin_convertvector(v, bf16x2);
    return __builtin_bit_cast(unsigned, r);
}
__device__ __forceinline__ float rbf(float x) { return bflo(pk2(x, x)); }
__device__ __forceinline__ f32x4 mfma16(u32x4 a, u32x4 b, f32x4 c) {
    return __builtin_amdgcn_mfma_f32_16x16x32_bf16(__builtin_bit_cast(bf16x8, a), __builtin_bit_cast(bf16x8, b), c, 0, 0, 0);
}

constexpr int PACK_GU_THREADS = 2 * 64 * 688;
constexpr int PACK_D_THREADS  = 172 * 256;
constexpr int PACK_BLOCKS = (PACK_GU_THREADS + PACK_D_THREADS) / 256;

__device__ __forceinline__ void pack_item(const u16* __restrict__ src, int N, int ko, int np, u32x4* __restrict__ dst0) {
    unsigned w[8];
#pragma unroll
    for (int j = 0; j < 8; ++j) w[j] = *reinterpret_cast<const unsigned*>(src + (size_t)(8 * ko + j) * N + 2 * np);
    u32x4 o0, o1;
#pragma unroll
    for (int c = 0; c < 4; ++c) {
        o0[c] = (w[2 * c] & 0xffffu) | (w[2 * c + 1] << 16);
        o1[c] = (w[2 * c] >> 16) | (w[2 * c + 1] & 0xffff0000u);
    }
    dst0[0] = o0;
    dst0[1] = o1;
}

__global__ __launch_bounds__(256) void prep_kernel(const u16* __restrict__ k, const u16* __restrict__ v,
                                                   const u16* __restrict__ shift_k,
                                                   const u16* __restrict__ wg, const u16* __restrict__ wu,
                                                   const u16* __restrict__ wd,
                                                   u16* __restrict__ kvR, u32x4* __restrict__ Wgu,
                                                   u32x4* __restrict__ Wd) {
    __shared__ unsigned tile[64 * 33];
    const int t = threadIdx.x;
    if (blockIdx.x < 256) {
        const int st = blockIdx.x >> 3, dt = blockIdx.x & 7;
        const int s0 = st * 64, d0 = dt * 64;
        const int c8 = t & 7, r = t >> 3;
        const int sA = s0 + 2 * r;
        const int dd = d0 + 8 * c8;
        u32x4 kr[5], vr[2], wv[4];
#pragma unroll
        for (int i = 0; i < 5; ++i) {
            const int s = sA - 3 + i;
            u32x4 z = {0u, 0u, 0u, 0u};
            kr[i] = (s >= 0) ? *reinterpret_cast<const u32x4*>(k + (size_t)s * D_ + dd) : z;
        }
#pragma unroll
        for (int i = 0; i < 2; ++i) vr[i] = *reinterpret_cast<const u32x4*>(v + (size_t)(sA + i) * D_ + dd);
#pragma unroll
        for (int i = 0; i < 4; ++i) wv[i] = *reinterpret_cast<const u32x4*>(shift_k + (size_t)dd * 4 + 8 * i);
#pragma unroll
        for (int j = 0; j < 8; ++j) {
            const unsigned wa = wv[j >> 1][(j & 1) * 2], wb = wv[j >> 1][(j & 1) * 2 + 1];
            const float w0 = bflo(wa), w1 = bfhi(wa), w2 = bflo(wb), w3 = bfhi(wb);
            float kk[5];
#pragma unroll
            for (int i = 0; i < 5; ++i) kk[i] = (j & 1) ? bfhi(kr[i][j >> 1]) : bflo(kr[i][j >> 1]);
            float res[2];
#pragma unroll
            for (int q = 0; q < 2; ++q) {
                float acc = rbf(kk[q] * w3);
                acc = rbf(acc + rbf(kk[q + 1] * w2));
                acc = rbf(acc + rbf(kk[q + 2] * w1));
                acc = rbf(acc + rbf(kk[q + 3] * w0));
                const float vv = (j & 1) ? bfhi(vr[q][j >> 1]) : bflo(vr[q][j >> 1]);
                res[q] = acc * vv;
            }
            tile[(8 * c8 + j) * 33 + (31 - r)] = pk2(res[1], res[0]);
        }
        __syncthreads();
#pragma unroll
        for (int i = 0; i < 2; ++i) {
            const int idx = t + 256 * i;
            const int d = idx >> 3, q = idx & 7;
            u32x4 o;
#pragma unroll
            for (int c = 0; c < 4; ++c) o[c] = tile[d * 33 + 4 * q + c];
            *reinterpret_cast<u32x4*>(kvR + (size_t)(d0 + d) * S_ + (S_ - 64 - s0) + 8 * q) = o;
        }
    } else {
        int id = (blockIdx.x - 256) * 256 + t;
        if (id < PACK_GU_THREADS) {
            const int np = id % 688;
            const int rest = id / 688;
            const int ko = rest & 63, m = rest >> 6;
            const int n = 2 * np;
            const int ft = n >> 4, ks = ko >> 2, g = ko & 3;
            pack_item(m ? wu : wg, F_, ko, np, Wgu + ((size_t)((ft * KS1 + ks) * 2 + m) * 64 + g * 16 + (n & 15)));
        } else {
            id -= PACK_GU_THREADS;
            const int np = id & 255;
            const int ko = id >> 8;
            const int n = 2 * np;
            const int nt = n >> 4, ks = ko >> 2, g = ko & 3;
            pack_item(wd, D_, ko, np, Wd + ((size_t)(nt * KS2 + ks) * 64 + g * 16 + (n & 15)));
        }
    }
}

constexpr int CV_O = 0;
constexpr int CV_E = 4160;
constexpr int CV_U = 8320;
constexpr int CV_CH = 12928;
constexpr int CV_Y = 4 * CV_CH;

__global__ __launch_bounds__(256) void conv_kernel(const u16* __restrict__ kvR, const u16* __restrict__ ssm_k,
                                                   const u16* __restrict__ ssm_D, u16* __restrict__ Y4) {
    __shared__ __attribute__((aligned(16))) char smem[CV_Y + 8192];
    const int t = threadIdx.x, l = t & 63;
    const int w = __builtin_amdgcn_readfirstlane(t >> 6);
    const int cq = blockIdx.x >> 1, half = blockIdx.x & 1;
    const int ch = 4 * cq + w;
    char* base = smem + w * CV_CH;
    {
        const u16* hp = ssm_k + (size_t)ch * S_;
        const u16* up = kvR + (size_t)ch * S_;
        u32x4 hv[4], uv[4];
        unsigned hx[4];
#pragma unroll
        for (int i = 0; i < 4; ++i) {
            const int m = l + 64 * i;
            hv[i] = *reinterpret_cast<const u32x4*>(hp + 8 * m);
            uv[i] = *reinterpret_cast<const u32x4*>(up + 8 * m);
            hx[i] = (m < 255) ? *reinterpret_cast<const unsigned*>(hp + 8 * m + 8) : 0u;
        }
        const u32x4 z = {0u, 0u, 0u, 0u};
        if (l < 32) *reinterpret_cast<u32x4*>(base + CV_U + 4096 + 16 * l) = z;
        if (l < 2) *reinterpret_cast<u32x4*>(base + CV_E + 16 * l) = z;
        if (l == 0) *reinterpret_cast<u32x4*>(base + CV_O) = z;
        if (l == 1) {
            u32x4 o = z;
            o[3] = ((unsigned)hp[0]) << 16;
            *reinterpret_cast<u32x4*>(base + CV_O + 16) = o;
        }
#pragma unroll
        for (int i = 0; i < 4; ++i) {
            const int m = l + 64 * i;
            u32x4 o;
            o[0] = (hv[i][0] >> 16) | (hv[i][1] << 16);
            o[1] = (hv[i][1] >> 16) | (hv[i][2] << 16);
            o[2] = (hv[i][2] >> 16) | (hv[i][3] << 16);
            o[3] = (hv[i][3] >> 16) | (hx[i] << 16);
            *reinterpret_cast<u32x4*>(base + CV_E + 32 + 16 * m) = hv[i];
            *reinterpret_cast<u32x4*>(base + CV_O + 32 + 16 * m) = o;
            *reinterpret_cast<u32x4*>(base + CV_U + 16 * m) = uv[i];
        }
    }
    __syncthreads();

    const int n0 = half ? 6 : 7, n1 = half ? 4 : 5, n2 = half ? 3 : 2, n3 = half ? 1 : 0;
    const int o0 = (l & 15) + 8 * (l >> 4);
    const unsigned* fp = reinterpret_cast<const unsigned*>(base + ((l & 1) ? (CV_E + 2 * (o0 + 1)) : (CV_O + 2 * o0)));
    const char* gp = base + CV_U + 2 * (2032 - 16 * (l & 15) + 8 * (l >> 4));
    f32x4 a0 = {0.f, 0.f, 0.f, 0.f}, a1 = a0, a2 = a0, a3 = a0;
    const int e3 = 8 * n3 + 8, e2 = 8 * n2 + 8, e1 = 8 * n1 + 8, e0 = 8 * n0 + 8;
#define LOADF(kk) u32x4 f; f[0] = fp[16 * (kk)]; f[1] = fp[16 * (kk) + 1]; f[2] = fp[16 * (kk) + 2]; f[3] = fp[16 * (kk) + 3];
#define LOADG(kk, n) (*reinterpret_cast<const u32x4*>(gp + 64 * ((kk) - 8 * (n))))
    int kk = 0;
#pragma unroll 4
    for (; kk < e3; ++kk) {
        LOADF(kk);
        a0 = mfma16(f, LOADG(kk, n0), a0);
        a1 = mfma16(f, LOADG(kk, n1), a1);
        a2 = mfma16(f, LOADG(kk, n2), a2);
        a3 = mfma16(f, LOADG(kk, n3), a3);
    }
#pragma unroll 4
    for (; kk < e2; ++kk) {
        LOADF(kk);
        a0 = mfma16(f, LOADG(kk, n0), a0);
        a1 = mfma16(f, LOADG(kk, n1), a1);
        a2 = mfma16(f, LOADG(kk, n2), a2);
    }
#pragma unroll 4
    for (; kk < e1; ++kk) {
        LOADF(kk);
        a0 = mfma16(f, LOADG(kk, n0), a0);
        a1 = mfma16(f, LOADG(kk, n1), a1);
    }
#pragma unroll 4
    for (; kk < e0; ++kk) {
        LOADF(kk);
        a0 = mfma16(f, LOADG(kk, n0), a0);
    }
    const float Dv = bflo((unsigned)ssm_D[ch]);
    u16* yb = reinterpret_cast<u16*>(smem + CV_Y);
    const int sl = 16 * (l & 15) + 4 * (l >> 4);
#define EPI(acc, n, i)                                                                        \
    {                                                                                         \
        const int s = 256 * (n) + sl;                                                         \
        const u32x2 uu = *reinterpret_cast<const u32x2*>(base + CV_U + 2 * (2044 - s));       \
        const float u0 = bfhi(uu[1]), u1 = bflo(uu[1]), u2 = bfhi(uu[0]), u3 = bflo(uu[0]);   \
        const unsigned p01 = pk2(rbf(acc[0]) + rbf(u0 * Dv), rbf(acc[1]) + rbf(u1 * Dv));     \
        const unsigned p23 = pk2(rbf(acc[2]) + rbf(u2 * Dv), rbf(acc[3]) + rbf(u3 * Dv));     \
        u16* q = yb + ((i) * 256 + sl) * 4 + w;                                               \
        q[0] = (u16)p01; q[4] = (u16)(p01 >> 16); q[8] = (u16)p23; q[12] = (u16)(p23 >> 16);  \
    }
    EPI(a0, n0, 0) EPI(a1, n1, 1) EPI(a2, n2, 2) EPI(a3, n3, 3)
    __syncthreads();
    {
        const u32x2* yr = reinterpret_cast<const u32x2*>(smem + CV_Y);
        u32x2* dst = reinterpret_cast<u32x2*>(Y4) + (size_t)cq * S_;
        dst[256 * n0 + t] = yr[t];
        dst[256 * n1 + t] = yr[256 + t];
        dst[256 * n2 + t] = yr[512 + t];
        dst[256 * n3 + t] = yr[768 + t];
    }
}

__device__ __forceinline__ float rowsum8(float v) {
    v += __shfl_xor(v, 8);
    v += __shfl_xor(v, 16);
    v += __shfl_xor(v, 32);
    return v;
}

constexpr int G1_PF = 8;
constexpr int G1_PF0 = 2;

__global__ __launch_bounds__(768, 3) void g1_kernel(const u16* __restrict__ x, const u32x2* __restrict__ Y4,
                                                    const u32x4* __restrict__ Wgu, u16* __restrict__ x1out,
                                                    u32x2* __restrict__ hpk) {
    __shared__ u32x4 At[4 * KS1 * 64];
    const int b = blockIdx.x;
    const int rb = (b & 7) * 4 + (b >> 6), cb = (b >> 3) & 7;
    const int l = threadIdx.x & 63;
    const int w = __builtin_amdgcn_readfirstlane(threadIdx.x >> 6);
    const int ntl = cb < 6 ? 11 : 10;
    const int ft = (cb < 6 ? 11 * cb : 66 + 10 * (cb - 6)) + w;
    const bool active = w < ntl;
    const u32x4* wp = Wgu + (size_t)(active ? ft : 0) * (KS1 * 2 * 64) + l;
    u32x4 wf[G1_PF][2];
#pragma unroll
    for (int i = 0; i < G1_PF0; ++i) {
        wf[i][0] = wp[(2 * i) * 64];
        wf[i][1] = wp[(2 * i + 1) * 64];
    }
    if (w < 8) {
        const int i = l & 7, g = l >> 3;
        const int row = rb * 64 + 8 * w + i;
        u32x4 xv[8], yv[8];
#pragma unroll
        for (int c = 0; c < 8; ++c) {
            const int d = 64 * c + 8 * g;
            xv[c] = *reinterpret_cast<const u32x4*>(x + (size_t)row * D_ + d);
            const u32x2 q0 = Y4[(size_t)(d >> 2) * S_ + row];
            const u32x2 q1 = Y4[(size_t)((d >> 2) + 1) * S_ + row];
            yv[c][0] = q0[0]; yv[c][1] = q0[1]; yv[c][2] = q1[0]; yv[c][3] = q1[1];
        }
        float xf[64];
#pragma unroll
        for (int c = 0; c < 8; ++c)
#pragma unroll
            for (int j = 0; j < 4; ++j) {
                xf[8 * c + 2 * j] = bflo(xv[c][j]);
                xf[8 * c + 2 * j + 1] = bfhi(xv[c][j]);
            }
        float s = 0.f;
#pragma unroll
        for (int e = 0; e < 64; ++e) s += xf[e];
        float mean = rowsum8(s) * (1.0f / 512.0f);
        float ss = 0.f;
#pragma unroll
        for (int e = 0; e < 64; ++e) { const float dv = xf[e] - mean; ss += dv * dv; }
        float var = rowsum8(ss) * (1.0f / 512.0f);
        float mb = rbf(mean);
        float inv = 1.0f / rbf(sqrtf(rbf(rbf(var) + 1e-5f)));
#pragma unroll
        for (int c = 0; c < 8; ++c) {
            u32x4 o;
#pragma unroll
            for (int j = 0; j < 4; ++j) {
                const int e = 8 * c + 2 * j;
                unsigned p = pk2(xf[e] - mb, xf[e + 1] - mb);
                p = pk2(bflo(p) * inv, bfhi(p) * inv);
                p = pk2(bflo(p) * bflo(yv[c][j]), bfhi(p) * bfhi(yv[c][j]));
                p = pk2(xf[e] + bflo(p), xf[e + 1] + bfhi(p));
                o[j] = p;
                xf[e] = bflo(p);
                xf[e + 1] = bfhi(p);
            }
            if (c == cb) *reinterpret_cast<u32x4*>(x1out + (size_t)row * D_ + 64 * c + 8 * g) = o;
        }
        s = 0.f;
#pragma unroll
        for (int e = 0; e < 64; ++e) s += xf[e];
        mean = rowsum8(s) * (1.0f / 512.0f);
        ss = 0.f;
#pragma unroll
        for (int e = 0; e < 64; ++e) { const float dv = xf[e] - mean; ss += dv * dv; }
        var = rowsum8(ss) * (1.0f / 512.0f);
        mb = rbf(mean);
        inv = 1.0f / rbf(sqrtf(rbf(rbf(var) + 1e-5f)));
        const int rt = w >> 1, s16 = 8 * (w & 1) + i;
#pragma unroll
        for (int c = 0; c < 8; ++c) {
            u32x4 o;
#pragma unroll
            for (int j = 0; j < 4; ++j) {
                const int e = 8 * c + 2 * j;
                unsigned p = pk2(xf[e] - mb, xf[e + 1] - mb);
                o[j] = pk2(bflo(p) * inv, bfhi(p) * inv);
            }
            At[(rt * KS1 + 2 * c + (g >> 2)) * 64 + (g & 3) * 16 + s16] = o;
        }
    }
#pragma unroll
    for (int i = G1_PF0; i < G1_PF; ++i) {
        wf[i][0] = wp[(2 * i) * 64];
        wf[i][1] = wp[(2 * i + 1) * 64];
    }
    __syncthreads();
    if (!active) return;

    f32x4 ag[4], au[4];
#pragma unroll
    for (int r = 0; r < 4; ++r) { ag[r] = (f32x4){0.f, 0.f, 0.f, 0.f}; au[r] = ag[r]; }
#pragma unroll
    for (int ks = 0; ks < KS1; ++ks) {
        const u32x4 wg = wf[ks % G1_PF][0], wu = wf[ks % G1_PF][1];
        if (ks + G1_PF < KS1) {
            wf[ks % G1_PF][0] = wp[(2 * (ks + G1_PF)) * 64];
            wf[ks % G1_PF][1] = wp[(2 * (ks + G1_PF) + 1) * 64];
        }
#pragma unroll
        for (int rt = 0; rt < 4; ++rt) {
            const u32x4 bb = At[(rt * KS1 + ks) * 64 + l];
            ag[rt] = mfma16(wg, bb, ag[rt]);
            au[rt] = mfma16(wu, bb, au[rt]);
        }
    }
    const int ks2 = ft >> 1;
    const int slot = (2 * (ft & 1) + (l >> 5)) * 16 + (l & 15);
#pragma unroll
    for (int rt = 0; rt < 4; ++rt) {
        float hv[4];
#pragma unroll
        for (int r = 0; r < 4; ++r) {
            const float gv = rbf(ag[rt][r]);
            const float uv = rbf(au[rt][r]);
            const float sg = rbf(gv / (1.0f + __expf(-gv)));
            hv[r] = sg * uv;
        }
        u32x2 o;
        o[0] = pk2(hv[0], hv[1]);
        o[1] = pk2(hv[2], hv[3]);
        hpk[((size_t)((rb * 4 + rt) * KS2 + ks2) * 64 + slot) * 2 + ((l >> 4) & 1)] = o;
    }
}

__global__ __launch_bounds__(512, 2) void g2_kernel(const u32x4* __restrict__ hpk, const u32x4* __restrict__ Wd,
                                                    const u16* __restrict__ xin, u16* __restrict__ xout) {
    __shared__ f32x4 red[8 * 16 * 64];
    const int b = blockIdx.x;
    const int rb = (b & 7) * 4 + (b >> 6), cb = (b >> 3) & 7;
    const int l = threadIdx.x & 63;
    const int w = __builtin_amdgcn_readfirstlane(threadIdx.x >> 6);
    const int start = 5 * w + (w < 3 ? w : 3);
    const bool six = w < 3;
    const u32x4* hp = hpk + ((size_t)(rb * 4) * KS2 + start) * 64 + l;
    const u32x4* wp = Wd + ((size_t)(cb * 4) * KS2 + start) * 64 + l;
    f32x4 acc[4][4];
#pragma unroll
    for (int i = 0; i < 4; ++i)
#pragma unroll
        for (int j = 0; j < 4; ++j) acc[i][j] = (f32x4){0.f, 0.f, 0.f, 0.f};
    u32x4 af[3][4], bf[3][4];
#pragma unroll
    for (int st = 0; st < 3; ++st)
#pragma unroll
        for (int i = 0; i < 4; ++i) {
            af[st][i] = wp[(i * KS2 + st) * 64];
            bf[st][i] = hp[(i * KS2 + st) * 64];
        }
#pragma unroll
    for (int st = 0; st < 6; ++st) {
        if (st == 5 && !six) break;
        u32x4 ac[4], bc[4];
#pragma unroll
        for (int i = 0; i < 4; ++i) { ac[i] = af[st % 3][i]; bc[i] = bf[st % 3][i]; }
        if (st + 3 < 5 || (st + 3 == 5 && six)) {
#pragma unroll
            for (int i = 0; i < 4; ++i) {
                af[st % 3][i] = wp[(i * KS2 + st + 3) * 64];
                bf[st % 3][i] = hp[(i * KS2 + st + 3) * 64];
            }
        }
#pragma unroll
        for (int i = 0; i < 4; ++i)
#pragma unroll
            for (int j = 0; j < 4; ++j) acc[i][j] = mfma16(ac[i], bc[j], acc[i][j]);
    }
#pragma unroll
    for (int i = 0; i < 4; ++i)
#pragma unroll
        for (int j = 0; j < 4; ++j) red[(w * 16 + i * 4 + j) * 64 + l] = acc[i][j];
    __syncthreads();
#pragma unroll
    for (int q = 0; q < 2; ++q) {
        const int tile = 2 * w + q;
        const int nt = tile >> 2, rt = tile & 3;
        const int n = cb * 64 + 16 * nt + 4 * (l >> 4);
        const int s = rb * 64 + 16 * rt + (l & 15);
        const u32x2 xr = *reinterpret_cast<const u32x2*>(xin + (size_t)s * D_ + n);
        f32x4 sum = red[tile * 64 + l];
#pragma unroll
        for (int p = 1; p < 8; ++p) sum += red[(p * 16 + tile) * 64 + l];
        u32x2 o;
        o[0] = pk2(bflo(xr[0]) + rbf(sum[0]), bfhi(xr[0]) + rbf(sum[1]));
        o[1] = pk2(bflo(xr[1]) + rbf(sum[2]), bfhi(xr[1]) + rbf(sum[3]));
        *reinterpret_cast<u32x2*>(xout + (size_t)s * D_ + n) = o;
    }
}

extern "C" void kernel_launch(void* const* d_in, const int* in_sizes, int n_in,
                              void* d_out, int out_size, void* d_ws, size_t ws_size,
                              hipStream_t stream) {
    const u16* q       = (const u16*)d_in[0];
    const u16* k       = (const u16*)d_in[1];
    const u16* v       = (const u16*)d_in[2];
    const u16* shift_k = (const u16*)d_in[3];
    const u16* ssm_k   = (const u16*)d_in[4];
    const u16* ssm_D   = (const u16*)d_in[5];
    const u16* w_gate  = (const u16*)d_in[7];
    const u16* w_up    = (const u16*)d_in[8];
    const u16* w_down  = (const u16*)d_in[9];
    u16* out = (u16*)d_out;

    char* ws = (char*)d_ws;
    const size_t MB2 = (size_t)S_ * D_ * 2;
    u16*   kvR = (u16*)(ws);
    u16*   Y4  = (u16*)(ws + MB2);
    u16*   x1  = (u16*)(ws + 2 * MB2);
    u16*   x2  = (u16*)(ws + 3 * MB2);
    u16*   x3  = (u16*)(ws + 4 * MB2);
    u32x4* Wgu = (u32x4*)(ws + 5 * MB2);
    u32x4* Wd  = (u32x4*)(ws + 5 * MB2 + (size_t)NFT * KS1 * 2 * 1024);
    u32x4* hpk = (u32x4*)(ws + 5 * MB2 + (size_t)NFT * KS1 * 2 * 1024 + (size_t)32 * KS2 * 1024);

    prep_kernel<<<256 + PACK_BLOCKS, 256, 0, stream>>>(k, v, shift_k, w_gate, w_up, w_down, kvR, Wgu, Wd);
    conv_kernel<<<256, 256, 0, stream>>>(kvR, ssm_k, ssm_D, Y4);
    g1_kernel<<<256, 768, 0, stream>>>(q, (const u32x2*)Y4, Wgu, x1, (u32x2*)hpk);
    g2_kernel<<<256, 512, 0, stream>>>(hpk, Wd, x1, x2);
    g1_kernel<<<256, 768, 0, stream>>>(x2, (const u32x2*)Y4, Wgu, x3, (u32x2*)hpk);
    g2_kernel<<<256, 512, 0, stream>>>(hpk, Wd, x3, out);
}
```

```cpp
#include <hip/hip_runtime.h>
#include <hip/hip_bf16.h>
#include <stdint.h>

typedef __attribute__((ext_vector_type(8))) __bf16   bf16x8;
typedef __attribute__((ext_vector_type(2))) __bf16   bf16x2;
typedef __attribute__((ext_vector_type(4))) float    f32x4;
typedef __attribute__((ext_vector_type(2))) float    f32x2;
typedef __attribute__((ext_vector_type(4))) unsigned u32x4;
typedef __attribute__((ext_vector_type(2))) unsigned u32x2;
typedef unsigned short u16;

constexpr int S_ = 2048, D_ = 512, F_ = 1376;
constexpr int NFT = 86;
constexpr int KS1 = 16;
constexpr int KS2 = 43;

__device__ __forceinline__ float bflo(unsigned u) { return __builtin_bit_cast(float, u << 16); }
__device__ __forceinline__ float bfhi(unsigned u) { return __builtin_bit_cast(float, u & 0xffff0000u); }
__device__ __forceinline__ unsigned pk2(float lo, float hi) {
    f32x2 v = {lo, hi};
    bf16x2 r = __builtin_convertvector(v, bf16x2);
    return __builtin_bit_cast(unsigned, r);
}
__device__ __forceinline__ float rbf(float x) { return bflo(pk2(x, x)); }
__device__ __forceinline__ f32x4 mfma16(u32x4 a, u32x4 b, f32x4 c) {
    return __builtin_amdgcn_mfma_f32_16x16x32_bf16(__builtin_bit_cast(bf16x8, a), __builtin_bit_cast(bf16x8, b), c, 0, 0, 0);
}

template <int IMM> __device__ __forceinline__ u32x4 gld16(const void* sbase, unsigned voff) {
    u32x4 r;
    asm volatile("global_load_dwordx4 %0, %1, %2 offset:%3" : "=v"(r) : "v"(voff), "s"(sbase), "i"(IMM) : "memory");
    return r;
}
template <int IMM> __device__ __forceinline__ u32x2 gld8(const void* sbase, unsigned voff) {
    u32x2 r;
    asm volatile("global_load_dwordx2 %0, %1, %2 offset:%3" : "=v"(r) : "v"(voff), "s"(sbase), "i"(IMM) : "memory");
    return r;
}
template <int N> __device__ __forceinline__ void vm_wait2(u32x4& a, u32x4& b) {
    asm volatile("s_waitcnt vmcnt(%2)" : "+v"(a), "+v"(b) : "i"(N) : "memory");
}
template <int N> __device__ __forceinline__ void vm_wait8(u32x4 (&a)[4], u32x4 (&b)[4]) {
    asm volatile("s_waitcnt vmcnt(%8)"
                 : "+v"(a[0]), "+v"(a[1]), "+v"(a[2]), "+v"(a[3]), "+v"(b[0]), "+v"(b[1]), "+v"(b[2]), "+v"(b[3])
                 : "i"(N) : "memory");
}
template <int N> __device__ __forceinline__ void vm_wait8(u32x4 (&a)[8]) {
    asm volatile("s_waitcnt vmcnt(%8)"
                 : "+v"(a[0]), "+v"(a[1]), "+v"(a[2]), "+v"(a[3]), "+v"(a[4]), "+v"(a[5]), "+v"(a[6]), "+v"(a[7])
                 : "i"(N) : "memory");
}
template <int N> __device__ __forceinline__ void vm_wait16(u32x2 (&a)[16]) {
    asm volatile("s_waitcnt vmcnt(%16)"
                 : "+v"(a[0]), "+v"(a[1]), "+v"(a[2]), "+v"(a[3]), "+v"(a[4]), "+v"(a[5]), "+v"(a[6]), "+v"(a[7]),
                   "+v"(a[8]), "+v"(a[9]), "+v"(a[10]), "+v"(a[11]), "+v"(a[12]), "+v"(a[13]), "+v"(a[14]), "+v"(a[15])
                 : "i"(N) : "memory");
}
template <int N> __device__ __forceinline__ void vm_wait2(u32x2& a, u32x2& b) {
    asm volatile("s_waitcnt vmcnt(%2)" : "+v"(a), "+v"(b) : "i"(N) : "memory");
}

constexpr int PACK_GU_THREADS = 2 * 64 * 688;
constexpr int PACK_D_THREADS  = 172 * 256;
constexpr int PACK_BLOCKS = (PACK_GU_THREADS + PACK_D_THREADS) / 256;

__device__ __forceinline__ void pack_item(const u16* __restrict__ src, int N, int ko, int np, u32x4* __restrict__ dst0) {
    unsigned w[8];
#pragma unroll
    for (int j = 0; j < 8; ++j) w[j] = *reinterpret_cast<const unsigned*>(src + (size_t)(8 * ko + j) * N + 2 * np);
    u32x4 o0, o1;
#pragma unroll
    for (int c = 0; c < 4; ++c) {
        o0[c] = (w[2 * c] & 0xffffu) | (w[2 * c + 1] << 16);
        o1[c] = (w[2 * c] >> 16) | (w[2 * c + 1] & 0xffff0000u);
    }
    dst0[0] = o0;
    dst0[1] = o1;
}

__global__ __launch_bounds__(256) void prep_kernel(const u16* __restrict__ k, const u16* __restrict__ v,
                                                   const u16* __restrict__ shift_k,
                                                   const u16* __restrict__ wg, const u16* __restrict__ wu,
                                                   const u16* __restrict__ wd,
                                                   u16* __restrict__ kvR, u32x4* __restrict__ Wgu,
                                                   u32x4* __restrict__ Wd) {
    __shared__ unsigned tile[64 * 33];
    const int t = threadIdx.x;
    if (blockIdx.x < 256) {
        const int st = blockIdx.x >> 3, dt = blockIdx.x & 7;
        const int s0 = st * 64, d0 = dt * 64;
        const int c8 = t & 7, r = t >> 3;
        const int sA = s0 + 2 * r;
        const int dd = d0 + 8 * c8;
        u32x4 kr[5], vr[2], wv[4];
#pragma unroll
        for (int i = 0; i < 5; ++i) {
            const int s = sA - 3 + i;
            u32x4 z = {0u, 0u, 0u, 0u};
            kr[i] = (s >= 0) ? *reinterpret_cast<const u32x4*>(k + (size_t)s * D_ + dd) : z;
        }
#pragma unroll
        for (int i = 0; i < 2; ++i) vr[i] = *reinterpret_cast<const u32x4*>(v + (size_t)(sA + i) * D_ + dd);
#pragma unroll
        for (int i = 0; i < 4; ++i) wv[i] = *reinterpret_cast<const u32x4*>(shift_k + (size_t)dd * 4 + 8 * i);
#pragma unroll
        for (int j = 0; j < 8; ++j) {
            const unsigned wa = wv[j >> 1][(j & 1) * 2], wb = wv[j >> 1][(j & 1) * 2 + 1];
            const float w0 = bflo(wa), w1 = bfhi(wa), w2 = bflo(wb), w3 = bfhi(wb);
            float kk[5];
#pragma unroll
            for (int i = 0; i < 5; ++i) kk[i] = (j & 1) ? bfhi(kr[i][j >> 1]) : bflo(kr[i][j >> 1]);
            float res[2];
#pragma unroll
            for (int q = 0; q < 2; ++q) {
                float acc = rbf(kk[q] * w3);
                acc = rbf(acc + rbf(kk[q + 1] * w2));
                acc = rbf(acc + rbf(kk[q + 2] * w1));
                acc = rbf(acc + rbf(kk[q + 3] * w0));
                const float vv = (j & 1) ? bfhi(vr[q][j >> 1]) : bflo(vr[q][j >> 1]);
                res[q] = acc * vv;
            }
            tile[(8 * c8 + j) * 33 + (31 - r)] = pk2(res[1], res[0]);
        }
        __syncthreads();
#pragma unroll
        for (int i = 0; i < 2; ++i) {
            const int idx = t + 256 * i;
            const int d = idx >> 3, q = idx & 7;
            u32x4 o;
#pragma unroll
            for (int c = 0; c < 4; ++c) o[c] = tile[d * 33 + 4 * q + c];
            *reinterpret_cast<u32x4*>(kvR + (size_t)(d0 + d) * S_ + (S_ - 64 - s0) + 8 * q) = o;
        }
    } else {
        int id = (blockIdx.x - 256) * 256 + t;
        if (id < PACK_GU_THREADS) {
            const int np = id % 688;
            const int rest = id / 688;
            const int ko = rest & 63, m = rest >> 6;
            const int n = 2 * np;
            const int ft = n >> 4, ks = ko >> 2, g = ko & 3;
            pack_item(m ? wu : wg, F_, ko, np, Wgu + ((size_t)((ft * KS1 + ks) * 2 + m) * 64 + g * 16 + (n & 15)));
        } else {
            id -= PACK_GU_THREADS;
            const int np = id & 255;
            const int ko = id >> 8;
            const int n = 2 * np;
            const int nt = n >> 4, ks = ko >> 2, g = ko & 3;
            pack_item(wd, D_, ko, np, Wd + ((size_t)(nt * KS2 + ks) * 64 + g * 16 + (n & 15)));
        }
    }
}

constexpr int CV_O = 0;
constexpr int CV_E = 4160;
constexpr int CV_U = 8320;
constexpr int CV_CH = 12928;
constexpr int CV_Y = 4 * CV_CH;

template <int N>
__device__ __forceinline__ void conv_task(const unsigned* fp, const char* gp, f32x4& acc1, f32x4& acc0) {
    u32x4 Fr[3][8], Gr[2][8];
#pragma unroll
    for (int t = -1; t <= N + 1; ++t) {
        if (t + 1 <= N + 1) {
#pragma unroll
            for (int i = 0; i < 8; ++i) {
                const int kk = 8 * (t + 1) + i;
                Gr[(t + 1) & 1][i] = *reinterpret_cast<const u32x4*>(gp + 64 * (8 * (t - N) + i));
                u32x4 f;
                f[0] = fp[16 * kk]; f[1] = fp[16 * kk + 1]; f[2] = fp[16 * kk + 2]; f[3] = fp[16 * kk + 3];
                Fr[(t + 1) % 3][i] = f;
            }
        }
        __builtin_amdgcn_sched_barrier(0);
        if (t >= 0) {
#pragma unroll
            for (int i = 0; i < 8; ++i) {
                acc1 = mfma16(Fr[t % 3][i], Gr[t & 1][i], acc1);
                if (t >= 1) acc0 = mfma16(Fr[(t + 2) % 3][i], Gr[t & 1][i], acc0);
            }
        }
        __builtin_amdgcn_sched_barrier(0);
    }
}

__global__ __launch_bounds__(256) void conv_kernel(const u16* __restrict__ kvR, const u16* __restrict__ ssm_k,
                                                   const u16* __restrict__ ssm_D, u16* __restrict__ Y4) {
    __shared__ __attribute__((aligned(16))) char smem[CV_Y + 8192];
    const int t = threadIdx.x, l = t & 63;
    const int w = __builtin_amdgcn_readfirstlane(t >> 6);
    const int cq = blockIdx.x >> 1, half = blockIdx.x & 1;
    const int ch = 4 * cq + w;
    char* base = smem + w * CV_CH;
    {
        const u16* hp = ssm_k + (size_t)ch * S_;
        const u16* up = kvR + (size_t)ch * S_;
        u32x4 hv[4], uv[4];
        unsigned hx[4];
#pragma unroll
        for (int i = 0; i < 4; ++i) {
            const int m = l + 64 * i;
            hv[i] = *reinterpret_cast<const u32x4*>(hp + 8 * m);
            uv[i] = *reinterpret_cast<const u32x4*>(up + 8 * m);
            hx[i] = (m < 255) ? *reinterpret_cast<const unsigned*>(hp + 8 * m + 8) : 0u;
        }
        const u32x4 z = {0u, 0u, 0u, 0u};
        if (l < 32) *reinterpret_cast<u32x4*>(base + CV_U + 4096 + 16 * l) = z;
        if (l < 2) *reinterpret_cast<u32x4*>(base + CV_E + 16 * l) = z;
        if (l == 0) *reinterpret_cast<u32x4*>(base + CV_O) = z;
        if (l == 1) {
            u32x4 o = z;
            o[3] = ((unsigned)hp[0]) << 16;
            *reinterpret_cast<u32x4*>(base + CV_O + 16) = o;
        }
#pragma unroll
        for (int i = 0; i < 4; ++i) {
            const int m = l + 64 * i;
            u32x4 o;
            o[0] = (hv[i][0] >> 16) | (hv[i][1] << 16);
            o[1] = (hv[i][1] >> 16) | (hv[i][2] << 16);
            o[2] = (hv[i][2] >> 16) | (hv[i][3] << 16);
            o[3] = (hv[i][3] >> 16) | (hx[i] << 16);
            *reinterpret_cast<u32x4*>(base + CV_E + 32 + 16 * m) = hv[i];
            *reinterpret_cast<u32x4*>(base + CV_O + 32 + 16 * m) = o;
            *reinterpret_cast<u32x4*>(base + CV_U + 16 * m) = uv[i];
        }
    }
    __syncthreads();

    const int o0 = (l & 15) + 8 * (l >> 4);
    const unsigned* fp = reinterpret_cast<const unsigned*>(base + ((l & 1) ? (CV_E + 2 * (o0 + 1)) : (CV_O + 2 * o0)));
    const char* gp = base + CV_U + 2 * (2032 - 16 * (l & 15) + 8 * (l >> 4));
    f32x4 a0 = {0.f, 0.f, 0.f, 0.f}, a1 = a0, a2 = a0, a3 = a0;
    int n0, n1, n2, n3;
    if (half == 0) {
        n0 = 7; n1 = 6; n2 = 1; n3 = 0;
        conv_task<6>(fp, gp, a0, a1);
        conv_task<0>(fp, gp, a2, a3);
    } else {
        n0 = 5; n1 = 4; n2 = 3; n3 = 2;
        conv_task<4>(fp, gp, a0, a1);
        conv_task<2>(fp, gp, a2, a3);
    }
    const float Dv = bflo((unsigned)ssm_D[ch]);
    u16* yb = reinterpret_cast<u16*>(smem + CV_Y);
    const int sl = 16 * (l & 15) + 4 * (l >> 4);
#define EPI(acc, n, i)                                                                        \
    {                                                                                         \
        const int s = 256 * (n) + sl;                                                         \
        const u32x2 uu = *reinterpret_cast<const u32x2*>(base + CV_U + 2 * (2044 - s));       \
        const float u0 = bfhi(uu[1]), u1 = bflo(uu[1]), u2 = bfhi(uu[0]), u3 = bflo(uu[0]);   \
        const unsigned p01 = pk2(rbf(acc[0]) + rbf(u0 * Dv), rbf(acc[1]) + rbf(u1 * Dv));     \
        const unsigned p23 = pk2(rbf(acc[2]) + rbf(u2 * Dv), rbf(acc[3]) + rbf(u3 * Dv));     \
        u16* q = yb + ((i) * 256 + sl) * 4 + w;                                               \
        q[0] = (u16)p01; q[4] = (u16)(p01 >> 16); q[8] = (u16)p23; q[12] = (u16)(p23 >> 16);  \
    }
    EPI(a0, n0, 0) EPI(a1, n1, 1) EPI(a2, n2, 2) EPI(a3, n3, 3)
    __syncthreads();
    {
        const u32x2* yr = reinterpret_cast<const u32x2*>(smem + CV_Y);
        u32x2* dst = reinterpret_cast<u32x2*>(Y4) + (size_t)cq * S_;
        dst[256 * n0 + t] = yr[t];
        dst[256 * n1 + t] = yr[256 + t];
        dst[256 * n2 + t] = yr[512 + t];
        dst[256 * n3 + t] = yr[768 + t];
    }
}

__device__ __forceinline__ float rowsum8(float v) {
    v += __shfl_xor(v, 8);
    v += __shfl_xor(v, 16);
    v += __shfl_xor(v, 32);
    return v;
}

constexpr int G1_WIN = 12;

__global__ __launch_bounds__(768, 3) void g1_kernel(const u16* __restrict__ x, const u32x2* __restrict__ Y4,
                                                    const u32x4* __restrict__ Wgu, u16* __restrict__ x1out,
                                                    u32x2* __restrict__ hpk) {
    __shared__ u32x4 At[4 * KS1 * 64];
    const int b = blockIdx.x;
    const int rb = (b & 7) * 4 + (b >> 6), cb = (b >> 3) & 7;
    const int l = threadIdx.x & 63;
    const int w = __builtin_amdgcn_readfirstlane(threadIdx.x >> 6);
    const int ntl = cb < 6 ? 11 : 10;
    const int ft = (cb < 6 ? 11 * cb : 66 + 10 * (cb - 6)) + w;
    const bool active = w < ntl;
    const u32x4* wbase = Wgu + (size_t)(active ? ft : 0) * (KS1 * 2 * 64);
    const unsigned wo = 16 * l;
    u32x4 wf[G1_WIN][2];
#define G1_LOADW(ks)                                                                  \
    {                                                                                 \
        wf[(ks) % G1_WIN][0] = gld16<((2 * (ks)) & 3) * 1024>(wbase, wo + ((2 * (ks)) >> 2) * 4096);         \
        wf[(ks) % G1_WIN][1] = gld16<((2 * (ks) + 1) & 3) * 1024>(wbase, wo + ((2 * (ks)) >> 2) * 4096);     \
    }
    G1_LOADW(0) G1_LOADW(1)
    if (w < 8) {
        const int i = l & 7, g = l >> 3;
        const int row = rb * 64 + 8 * w + i;
        u32x4 xv[8];
        u32x2 yq[16];
        const unsigned xo = row * 1024 + 16 * g;
        xv[0] = gld16<0>(x, xo);   xv[1] = gld16<128>(x, xo); xv[2] = gld16<256>(x, xo); xv[3] = gld16<384>(x, xo);
        xv[4] = gld16<512>(x, xo); xv[5] = gld16<640>(x, xo); xv[6] = gld16<768>(x, xo); xv[7] = gld16<896>(x, xo);
#pragma unroll
        for (int c = 0; c < 8; ++c) {
            const unsigned yo = (16 * c + 2 * g) * 16384 + row * 8;
            yq[2 * c] = gld8<0>(Y4, yo);
            yq[2 * c + 1] = gld8<0>(Y4, yo + 16384);
        }
        vm_wait8<16>(xv);
        float xf[64];
#pragma unroll
        for (int c = 0; c < 8; ++c)
#pragma unroll
            for (int j = 0; j < 4; ++j) {
                xf[8 * c + 2 * j] = bflo(xv[c][j]);
                xf[8 * c + 2 * j + 1] = bfhi(xv[c][j]);
            }
        float s = 0.f;
#pragma unroll
        for (int e = 0; e < 64; ++e) s += xf[e];
        float mean = rowsum8(s) * (1.0f / 512.0f);
        float ss = 0.f;
#pragma unroll
        for (int e = 0; e < 64; ++e) { const float dv = xf[e] - mean; ss += dv * dv; }
        float var = rowsum8(ss) * (1.0f / 512.0f);
        float mb = rbf(mean);
        float inv = 1.0f / rbf(sqrtf(rbf(rbf(var) + 1e-5f)));
        vm_wait16<0>(yq);
#pragma unroll
        for (int c = 0; c < 8; ++c) {
            u32x4 o;
#pragma unroll
            for (int j = 0; j < 4; ++j) {
                const int e = 8 * c + 2 * j;
                const unsigned yy = yq[2 * c + (j >> 1)][j & 1];
                unsigned p = pk2(xf[e] - mb, xf[e + 1] - mb);
                p = pk2(bflo(p) * inv, bfhi(p) * inv);
                p = pk2(bflo(p) * bflo(yy), bfhi(p) * bfhi(yy));
                p = pk2(xf[e] + bflo(p), xf[e + 1] + bfhi(p));
                o[j] = p;
                xf[e] = bflo(p);
                xf[e + 1] = bfhi(p);
            }
            if (c == cb) *reinterpret_cast<u32x4*>(x1out + (size_t)row * D_ + 64 * c + 8 * g) = o;
        }
        s = 0.f;
#pragma unroll
        for (int e = 0; e < 64; ++e) s += xf[e];
        mean = rowsum8(s) * (1.0f / 512.0f);
        ss = 0.f;
#pragma unroll
        for (int e = 0; e < 64; ++e) { const float dv = xf[e] - mean; ss += dv * dv; }
        var = rowsum8(ss) * (1.0f / 512.0f);
        mb = rbf(mean);
        inv = 1.0f / rbf(sqrtf(rbf(rbf(var) + 1e-5f)));
        const int rt = w >> 1, s16 = 8 * (w & 1) + i;
#pragma unroll
        for (int c = 0; c < 8; ++c) {
            u32x4 o;
#pragma unroll
            for (int j = 0; j < 4; ++j) {
                const int e = 8 * c + 2 * j;
                unsigned p = pk2(xf[e] - mb, xf[e + 1] - mb);
                o[j] = pk2(bflo(p) * inv, bfhi(p) * inv);
            }
            At[(rt * KS1 + 2 * c + (g >> 2)) * 64 + (g & 3) * 16 + s16] = o;
        }
    }
    G1_LOADW(2) G1_LOADW(3) G1_LOADW(4) G1_LOADW(5) G1_LOADW(6)
    G1_LOADW(7) G1_LOADW(8) G1_LOADW(9) G1_LOADW(10) G1_LOADW(11)
    __syncthreads();
    if (!active) return;

    f32x4 ag[4], au[4];
#pragma unroll
    for (int r = 0; r < 4; ++r) { ag[r] = (f32x4){0.f, 0.f, 0.f, 0.f}; au[r] = ag[r]; }
#define G1_STEP(ks)                                                                   \
    {                                                                                 \
        constexpr int issued = 2 * G1_WIN + 2 * ((ks) < (KS1 - G1_WIN) ? (ks) : (KS1 - G1_WIN)); \
        vm_wait2<issued - 2 * (ks) - 2>(wf[(ks) % G1_WIN][0], wf[(ks) % G1_WIN][1]);   \
        _Pragma("unroll") for (int rt = 0; rt < 4; ++rt) {                            \
            const u32x4 bb = At[(rt * KS1 + (ks)) * 64 + l];                          \
            ag[rt] = mfma16(wf[(ks) % G1_WIN][0], bb, ag[rt]);                        \
            au[rt] = mfma16(wf[(ks) % G1_WIN][1], bb, au[rt]);                        \
        }                                                                             \
        if ((ks) + G1_WIN < KS1) G1_LOADW((ks) + G1_WIN)                              \
    }
    G1_STEP(0) G1_STEP(1) G1_STEP(2) G1_STEP(3) G1_STEP(4) G1_STEP(5) G1_STEP(6) G1_STEP(7)
    G1_STEP(8) G1_STEP(9) G1_STEP(10) G1_STEP(11) G1_STEP(12) G1_STEP(13) G1_STEP(14) G1_STEP(15)
    const int ks2 = ft >> 1;
    const int slot = (2 * (ft & 1) + (l >> 5)) * 16 + (l & 15);
#pragma unroll
    for (int rt = 0; rt < 4; ++rt) {
        float hv[4];
#pragma unroll
        for (int r = 0; r < 4; ++r) {
            const float gv = rbf(ag[rt][r]);
            const float uv = rbf(au[rt][r]);
            const float sg = rbf(gv / (1.0f + __expf(-gv)));
            hv[r] = sg * uv;
        }
        u32x2 o;
        o[0] = pk2(hv[0], hv[1]);
        o[1] = pk2(hv[2], hv[3]);
        hpk[((size_t)((rb * 4 + rt) * KS2 + ks2) * 64 + slot) * 2 + ((l >> 4) & 1)] = o;
    }
}

template <bool SIX>
__device__ __forceinline__ void g2_main(const u32x4* hbase, const u32x4* wbase, int l, f32x4 (&acc)[4][4]) {
    u32x4 af[5][4], bf[5][4];
    unsigned vo[4];
#pragma unroll
    for (int i = 0; i < 4; ++i) vo[i] = 16 * l + i * (KS2 * 1024);
#define G2_LOAD(st)                                                                                   \
    _Pragma("unroll") for (int i = 0; i < 4; ++i) {                                                   \
        af[(st) % 5][i] = gld16<((st) & 3) * 1024>(wbase, vo[i] + ((st) >> 2) * 4096);                \
        bf[(st) % 5][i] = gld16<((st) & 3) * 1024>(hbase, vo[i] + ((st) >> 2) * 4096);                \
    }
#define G2_STEP(st, nwait)                                                                            \
    {                                                                                                 \
        vm_wait8<nwait>(af[(st) % 5], bf[(st) % 5]);                                                  \
        _Pragma("unroll") for (int i = 0; i < 4; ++i)                                                 \
            _Pragma("unroll") for (int j = 0; j < 4; ++j)                                             \
                acc[i][j] = mfma16(af[(st) % 5][i], bf[(st) % 5][j], acc[i][j]);                      \
    }
    G2_LOAD(0) G2_LOAD(1) G2_LOAD(2) G2_LOAD(3) G2_LOAD(4)
    G2_STEP(0, 32)
    if (SIX) {
        G2_LOAD(5)
        G2_STEP(1, 32) G2_STEP(2, 24) G2_STEP(3, 16) G2_STEP(4, 8) G2_STEP(5, 0)
    } else {
        G2_STEP(1, 24) G2_STEP(2, 16) G2_STEP(3, 8) G2_STEP(4, 0)
    }
}

__global__ __launch_bounds__(512, 2) void g2_kernel(const u32x4* __restrict__ hpk, const u32x4* __restrict__ Wd,
                                                    const u16* __restrict__ xin, u16* __restrict__ xout) {
    __shared__ f32x4 red[8 * 16 * 64];
    const int b = blockIdx.x;
    const int rb = (b & 7) * 4 + (b >> 6), cb = (b >> 3) & 7;
    const int l = threadIdx.x & 63;
    const int w = __builtin_amdgcn_readfirstlane(threadIdx.x >> 6);
    const int start = 5 * w + (w < 3 ? w : 3);
    const u32x4* hbase = hpk + ((size_t)(rb * 4) * KS2 + start) * 64;
    const u32x4* wbase = Wd + ((size_t)(cb * 4) * KS2 + start) * 64;
    const int n = cb * 64 + 16 * (w >> 1) + 4 * (l >> 4);
    const int s0 = rb * 64 + 32 * (w & 1) + (l & 15);
    const unsigned xo = (unsigned)(s0 * D_ + n) * 2;
    u32x2 xr0 = gld8<0>(xin, xo), xr1 = gld8<0>(xin, xo + 16 * D_ * 2);
    f32x4 acc[4][4];
#pragma unroll
    for (int i = 0; i < 4; ++i)
#pragma unroll
        for (int j = 0; j < 4; ++j) acc[i][j] = (f32x4){0.f, 0.f, 0.f, 0.f};
    if (w < 3) g2_main<true>(hbase, wbase, l, acc);
    else g2_main<false>(hbase, wbase, l, acc);
#pragma unroll
    for (int i = 0; i < 4; ++i)
#pragma unroll
        for (int j = 0; j < 4; ++j) red[(w * 16 + i * 4 + j) * 64 + l] = acc[i][j];
    __syncthreads();
    vm_wait2<0>(xr0, xr1);
#pragma unroll
    for (int q = 0; q < 2; ++q) {
        const int tile = 2 * w + q;
        const u32x2 xr = q ? xr1 : xr0;
        f32x4 sum = red[tile * 64 + l];
#pragma unroll
        for (int p = 1; p < 8; ++p) sum += red[(p * 16 + tile) * 64 + l];
        u32x2 o;
        o[0] = pk2(bflo(xr[0]) + rbf(sum[0]), bfhi(xr[0]) + rbf(sum[1]));
        o[1] = pk2(bflo(xr[1]) + rbf(sum[2]), bfhi(xr[1]) + rbf(sum[3]));
        *reinterpret_cast<u32x2*>(xout + (size_t)(s0 + 16 * q) * D_ + n) = o;
    }
}

extern "C" void kernel_launch(void* const* d_in, const int* in_sizes, int n_in,
                              void* d_out, int out_size, void* d_ws, size_t ws_size,
                              hipStream_t stream) {
    const u16* q       = (const u16*)d_in[0];
    const u16* k       = (const u16*)d_in[1];
    const u16* v       = (const u16*)d_in[2];
    const u16* shift_k = (const u16*)d_in[3];
    const u16* ssm_k   = (const u16*)d_in[4];
    const u16* ssm_D   = (const u16*)d_in[5];
    const u16* w_gate  = (const u16*)d_in[7];
    const u16* w_up    = (const u16*)d_in[8];
    const u16* w_down  = (const u16*)d_in[9];
    u16* out = (u16*)d_out;

    char* ws = (char*)d_ws;
    const size_t MB2 = (size_t)S_ * D_ * 2;
    u16*   kvR = (u16*)(ws);
    u16*   Y4  = (u16*)(ws + MB2);
    u16*   x1  = (u16*)(ws + 2 * MB2);
    u16*   x2  = (u16*)(ws + 3 * MB2);
    u16*   x3  = (u16*)(ws + 4 * MB2);
    u32x4* Wgu = (u32x4*)(ws + 5 * MB2);
    u32x4* Wd  = (u32x4*)(ws + 5 * MB2 + (size_t)NFT * KS1 * 2 * 1024);
    u32x4* hpk = (u32x4*)(ws + 5 * MB2 + (size_t)NFT * KS1 * 2 * 1024 + (size_t)32 * KS2 * 1024);

    prep_kernel<<<256 + PACK_BLOCKS, 256, 0, stream>>>(k, v, shift_k, w_gate, w_up, w_down, kvR, Wgu, Wd);
    conv_kernel<<<256, 256, 0, stream>>>(kvR, ssm_k, ssm_D, Y4);
    g1_kernel<<<256, 768, 0, stream>>>(q, (const u32x2*)Y4, Wgu, x1, (u32x2*)hpk);
    g2_kernel<<<256, 512, 0, stream>>>(hpk, Wd, x1, x2);
    g1_kernel<<<256, 768, 0, stream>>>(x2, (const u32x2*)Y4, Wgu, x3, (u32x2*)hpk);
    g2_kernel<<<256, 512, 0, stream>>>(hpk, Wd, x3, out);
}
```

```cpp
#include <hip/hip_runtime.h>
#include <hip/hip_bf16.h>
#include <stdint.h>

typedef __attribute__((ext_vector_type(8))) __bf16   bf16x8;
typedef __attribute__((ext_vector_type(2))) __bf16   bf16x2;
typedef __attribute__((ext_vector_type(4))) float    f32x4;
typedef __attribute__((ext_vector_type(2))) float    f32x2;
typedef __attribute__((ext_vector_type(4))) unsigned u32x4;
typedef __attribute__((ext_vector_type(2))) unsigned u32x2;
typedef unsigned short u16;

#define STAMP(k, i)

constexpr int S_ = 2048, D_ = 512, F_ = 1376;
constexpr int NFT = 86;
constexpr int KS1 = 16;
constexpr int KS2 = 43;

__device__ __forceinline__ float bflo(unsigned u) { return __builtin_bit_cast(float, u << 16); }
__device__ __forceinline__ float bfhi(unsigned u) { return __builtin_bit_cast(float, u & 0xffff0000u); }
__device__ __forceinline__ unsigned pk2(float lo, float hi) {
    f32x2 v = {lo, hi};
    bf16x2 r = __builtin_convertvector(v, bf16x2);
    return __builtin_bit_cast(unsigned, r);
}
__device__ __forceinline__ float rbf(float x) { return bflo(pk2(x, x)); }
__device__ __forceinline__ f32x4 mfma16(u32x4 a, u32x4 b, f32x4 c) {
    return __builtin_amdgcn_mfma_f32_16x16x32_bf16(__builtin_bit_cast(bf16x8, a), __builtin_bit_cast(bf16x8, b), c, 0, 0, 0);
}

template <int IMM> __device__ __forceinline__ u32x4 gld16(const void* sbase, unsigned voff) {
    u32x4 r;
    asm volatile("global_load_dwordx4 %0, %1, %2 offset:%3" : "=v"(r) : "v"(voff), "s"(sbase), "i"(IMM) : "memory");
    return r;
}
template <int IMM> __device__ __forceinline__ u32x2 gld8(const void* sbase, unsigned voff) {
    u32x2 r;
    asm volatile("global_load_dwordx2 %0, %1, %2 offset:%3" : "=v"(r) : "v"(voff), "s"(sbase), "i"(IMM) : "memory");
    return r;
}
template <int N> __device__ __forceinline__ void vm_wait2(u32x4& a, u32x4& b) {
    asm volatile("s_waitcnt vmcnt(%2)" : "+v"(a), "+v"(b) : "i"(N) : "memory");
}
template <int N> __device__ __forceinline__ void vm_wait8(u32x4 (&a)[4], u32x4 (&b)[4]) {
    asm volatile("s_waitcnt vmcnt(%8)"
                 : "+v"(a[0]), "+v"(a[1]), "+v"(a[2]), "+v"(a[3]), "+v"(b[0]), "+v"(b[1]), "+v"(b[2]), "+v"(b[3])
                 : "i"(N) : "memory");
}
template <int N> __device__ __forceinline__ void vm_wait8(u32x4 (&a)[8]) {
    asm volatile("s_waitcnt vmcnt(%8)"
                 : "+v"(a[0]), "+v"(a[1]), "+v"(a[2]), "+v"(a[3]), "+v"(a[4]), "+v"(a[5]), "+v"(a[6]), "+v"(a[7])
                 : "i"(N) : "memory");
}
template <int N> __device__ __forceinline__ void vm_wait16(u32x2 (&a)[16]) {
    asm volatile("s_waitcnt vmcnt(%16)"
                 : "+v"(a[0]), "+v"(a[1]), "+v"(a[2]), "+v"(a[3]), "+v"(a[4]), "+v"(a[5]), "+v"(a[6]), "+v"(a[7]),
                   "+v"(a[8]), "+v"(a[9]), "+v"(a[10]), "+v"(a[11]), "+v"(a[12]), "+v"(a[13]), "+v"(a[14]), "+v"(a[15])
                 : "i"(N) : "memory");
}
template <int N> __device__ __forceinline__ void vm_wait1(u32x2& a) {
    asm volatile("s_waitcnt vmcnt(%1)" : "+v"(a) : "i"(N) : "memory");
}
template <int N> __device__ __forceinline__ void vm_wait2(u32x2& a, u32x2& b) {
    asm volatile("s_waitcnt vmcnt(%2)" : "+v"(a), "+v"(b) : "i"(N) : "memory");
}

constexpr int PACK_GU_THREADS = 2 * 64 * 688;
constexpr int PACK_D_THREADS  = 172 * 256;
constexpr int PACK_BLOCKS = (PACK_GU_THREADS + PACK_D_THREADS) / 256;

__device__ __forceinline__ void pack_item(const u16* __restrict__ src, int N, int ko, int np, u32x4* __restrict__ dst0) {
    unsigned w[8];
#pragma unroll
    for (int j = 0; j < 8; ++j) w[j] = *reinterpret_cast<const unsigned*>(src + (size_t)(8 * ko + j) * N + 2 * np);
    u32x4 o0, o1;
#pragma unroll
    for (int c = 0; c < 4; ++c) {
        o0[c] = (w[2 * c] & 0xffffu) | (w[2 * c + 1] << 16);
        o1[c] = (w[2 * c] >> 16) | (w[2 * c + 1] & 0xffff0000u);
    }
    dst0[0] = o0;
    dst0[1] = o1;
}

__global__ __launch_bounds__(256) void prep_kernel(const u16* __restrict__ k, const u16* __restrict__ v,
                                                   const u16* __restrict__ shift_k,
                                                   const u16* __restrict__ wg, const u16* __restrict__ wu,
                                                   const u16* __restrict__ wd,
                                                   const u16* __restrict__ x0,
                                                   u16* __restrict__ kvR, u32x4* __restrict__ Wgu,
                                                   u32x4* __restrict__ Wd, float* __restrict__ csum,
                                                   f32x2* __restrict__ part0) {
    __shared__ unsigned tile[64 * 33];
    const int t = threadIdx.x;
    STAMP(0, 0);
    if (blockIdx.x < 256) {
        const int st = blockIdx.x >> 3, dt = blockIdx.x & 7;
        const int s0 = st * 64, d0 = dt * 64;
        const int c8 = t & 7, r = t >> 3;
        const int sA = s0 + 2 * r;
        const int dd = d0 + 8 * c8;
        u32x4 kr[5], vr[2], wv[4];
#pragma unroll
        for (int i = 0; i < 5; ++i) {
            const int s = sA - 3 + i;
            u32x4 z = {0u, 0u, 0u, 0u};
            kr[i] = (s >= 0) ? *reinterpret_cast<const u32x4*>(k + (size_t)s * D_ + dd) : z;
        }
#pragma unroll
        for (int i = 0; i < 2; ++i) vr[i] = *reinterpret_cast<const u32x4*>(v + (size_t)(sA + i) * D_ + dd);
#pragma unroll
        for (int i = 0; i < 4; ++i) wv[i] = *reinterpret_cast<const u32x4*>(shift_k + (size_t)dd * 4 + 8 * i);
#pragma unroll
        for (int j = 0; j < 8; ++j) {
            const unsigned wa = wv[j >> 1][(j & 1) * 2], wb = wv[j >> 1][(j & 1) * 2 + 1];
            const float w0 = bflo(wa), w1 = bfhi(wa), w2 = bflo(wb), w3 = bfhi(wb);
            float kk[5];
#pragma unroll
            for (int i = 0; i < 5; ++i) kk[i] = (j & 1) ? bfhi(kr[i][j >> 1]) : bflo(kr[i][j >> 1]);
            float res[2];
#pragma unroll
            for (int q = 0; q < 2; ++q) {
                float acc = rbf(kk[q] * w3);
                acc = rbf(acc + rbf(kk[q + 1] * w2));
                acc = rbf(acc + rbf(kk[q + 2] * w1));
                acc = rbf(acc + rbf(kk[q + 3] * w0));
                const float vv = (j & 1) ? bfhi(vr[q][j >> 1]) : bflo(vr[q][j >> 1]);
                res[q] = acc * vv;
            }
            tile[(8 * c8 + j) * 33 + (31 - r)] = pk2(res[1], res[0]);
        }
        STAMP(0, 1);
        __syncthreads();
#pragma unroll
        for (int i = 0; i < 2; ++i) {
            const int idx = t + 256 * i;
            const int d = idx >> 3, q = idx & 7;
            u32x4 o;
#pragma unroll
            for (int c = 0; c < 4; ++c) o[c] = tile[d * 33 + 4 * q + c];
            *reinterpret_cast<u32x4*>(kvR + (size_t)(d0 + d) * S_ + (S_ - 64 - s0) + 8 * q) = o;
        }
    } else if (blockIdx.x >= 256 + PACK_BLOCKS + 86) {
        const int rbase = (blockIdx.x - (256 + PACK_BLOCKS + 86)) * 8 + 2 * (t >> 6);
        const int l = t & 63;
        u32x4 a[2];
#pragma unroll
        for (int i = 0; i < 2; ++i) a[i] = *reinterpret_cast<const u32x4*>(x0 + (size_t)(rbase + i) * D_ + 8 * l);
#pragma unroll
        for (int i = 0; i < 2; ++i) {
            float sm = 0.f, sq = 0.f;
#pragma unroll
            for (int j = 0; j < 4; ++j) {
                const float lo = bflo(a[i][j]), hi = bfhi(a[i][j]);
                sm += lo + hi;
                sq += lo * lo + hi * hi;
            }
#pragma unroll
            for (int m = 1; m < 8; m <<= 1) { sm += __shfl_xor(sm, m); sq += __shfl_xor(sq, m); }
            if ((l & 7) == 0) { f32x2 o = {sm, sq}; part0[(size_t)(rbase + i) * 8 + (l >> 3)] = o; }
        }
    } else if (blockIdx.x >= 256 + PACK_BLOCKS) {
        const int cbk = blockIdx.x - (256 + PACK_BLOCKS);
        const int m = cbk / 43, f0 = (cbk % 43) * 32;
        const u16* src = m ? wu : wg;
        const int np = t & 15, kg = t >> 4;
        unsigned wv[32];
#pragma unroll
        for (int j = 0; j < 32; ++j) wv[j] = *reinterpret_cast<const unsigned*>(src + (size_t)(32 * kg + j) * F_ + f0 + 2 * np);
        float s0 = 0.f, s1 = 0.f;
#pragma unroll
        for (int j = 0; j < 32; ++j) { s0 += bflo(wv[j]); s1 += bfhi(wv[j]); }
        float* red = reinterpret_cast<float*>(tile);
        red[kg * 33 + 2 * np] = s0;
        red[kg * 33 + 2 * np + 1] = s1;
        __syncthreads();
        if (t < 32) {
            float a = 0.f;
#pragma unroll
            for (int j = 0; j < 16; ++j) a += red[j * 33 + t];
            csum[m * F_ + f0 + t] = a;
        }
    } else {
        int id = (blockIdx.x - 256) * 256 + t;
        if (id < PACK_GU_THREADS) {
            const int np = id % 688;
            const int rest = id / 688;
            const int ko = rest & 63, m = rest >> 6;
            const int n = 2 * np;
            const int ft = n >> 4, ks = ko >> 2, g = ko & 3;
            pack_item(m ? wu : wg, F_, ko, np, Wgu + ((size_t)((ft * KS1 + ks) * 2 + m) * 64 + g * 16 + (n & 15)));
        } else {
            id -= PACK_GU_THREADS;
            const int np = id & 255;
            const int ko = id >> 8;
            const int n = 2 * np;
            const int nt = n >> 4, ks = ko >> 2, g = ko & 3;
            pack_item(wd, D_, ko, np, Wd + ((size_t)(nt * KS2 + ks) * 64 + g * 16 + (n & 15)));
        }
    }
    STAMP(0, 2);
}

constexpr int CV_O = 0;
constexpr int CV_E = 4160;
constexpr int CV_U = 8320;
constexpr int CV_CH = 12928;
constexpr int CV_Y = 4 * CV_CH;

template <int N>
__device__ __forceinline__ void conv_task(const unsigned* fp, const char* gp, f32x4& acc1, f32x4& acc0) {
    u32x4 Fr[3][8], Gr[2][8];
#pragma unroll
    for (int t = -1; t <= N + 1; ++t) {
        if (t + 1 <= N + 1) {
#pragma unroll
            for (int i = 0; i < 8; ++i) {
                const int kk = 8 * (t + 1) + i;
                Gr[(t + 1) & 1][i] = *reinterpret_cast<const u32x4*>(gp + 64 * (8 * (t - N) + i));
                u32x4 f;
                f[0] = fp[16 * kk]; f[1] = fp[16 * kk + 1]; f[2] = fp[16 * kk + 2]; f[3] = fp[16 * kk + 3];
                Fr[(t + 1) % 3][i] = f;
            }
        }
        __builtin_amdgcn_sched_barrier(0);
        if (t >= 0) {
#pragma unroll
            for (int i = 0; i < 8; ++i) {
                acc1 = mfma16(Fr[t % 3][i], Gr[t & 1][i], acc1);
                if (t >= 1) acc0 = mfma16(Fr[(t + 2) % 3][i], Gr[t & 1][i], acc0);
            }
        }
        __builtin_amdgcn_sched_barrier(0);
    }
}

__global__ __launch_bounds__(256) void conv_kernel(const u16* __restrict__ kvR, const u16* __restrict__ ssm_k,
                                                   const u16* __restrict__ ssm_D, u16* __restrict__ Y4) {
    __shared__ __attribute__((aligned(16))) char smem[CV_Y + 8192];
    const int t = threadIdx.x, l = t & 63;
    const int w = __builtin_amdgcn_readfirstlane(t >> 6);
    const int cq = blockIdx.x >> 1, half = blockIdx.x & 1;
    const int ch = 4 * cq + w;
    char* base = smem + w * CV_CH;
    STAMP(1, 0);
    {
        const u16* hp = ssm_k + (size_t)ch * S_;
        const u16* up = kvR + (size_t)ch * S_;
        u32x4 hv[4], uv[4];
        unsigned hx[4];
#pragma unroll
        for (int i = 0; i < 4; ++i) {
            const int m = l + 64 * i;
            hv[i] = *reinterpret_cast<const u32x4*>(hp + 8 * m);
            uv[i] = *reinterpret_cast<const u32x4*>(up + 8 * m);
            hx[i] = (m < 255) ? *reinterpret_cast<const unsigned*>(hp + 8 * m + 8) : 0u;
        }
        const u32x4 z = {0u, 0u, 0u, 0u};
        if (l < 32) *reinterpret_cast<u32x4*>(base + CV_U + 4096 + 16 * l) = z;
        if (l < 2) *reinterpret_cast<u32x4*>(base + CV_E + 16 * l) = z;
        if (l == 0) *reinterpret_cast<u32x4*>(base + CV_O) = z;
        if (l == 1) {
            u32x4 o = z;
            o[3] = ((unsigned)hp[0]) << 16;
            *reinterpret_cast<u32x4*>(base + CV_O + 16) = o;
        }
#pragma unroll
        for (int i = 0; i < 4; ++i) {
            const int m = l + 64 * i;
            u32x4 o;
            o[0] = (hv[i][0] >> 16) | (hv[i][1] << 16);
            o[1] = (hv[i][1] >> 16) | (hv[i][2] << 16);
            o[2] = (hv[i][2] >> 16) | (hv[i][3] << 16);
            o[3] = (hv[i][3] >> 16) | (hx[i] << 16);
            *reinterpret_cast<u32x4*>(base + CV_E + 32 + 16 * m) = hv[i];
            *reinterpret_cast<u32x4*>(base + CV_O + 32 + 16 * m) = o;
            *reinterpret_cast<u32x4*>(base + CV_U + 16 * m) = uv[i];
        }
    }
    STAMP(1, 1);
    __syncthreads();
    STAMP(1, 2);

    const int o0 = (l & 15) + 8 * (l >> 4);
    const unsigned* fp = reinterpret_cast<const unsigned*>(base + ((l & 1) ? (CV_E + 2 * (o0 + 1)) : (CV_O + 2 * o0)));
    const char* gp = base + CV_U + 2 * (2032 - 16 * (l & 15) + 8 * (l >> 4));
    f32x4 a0 = {0.f, 0.f, 0.f, 0.f}, a1 = a0, a2 = a0, a3 = a0;
    int n0, n1, n2, n3;
    if (half == 0) {
        n0 = 7; n1 = 6; n2 = 1; n3 = 0;
        conv_task<6>(fp, gp, a0, a1);
        conv_task<0>(fp, gp, a2, a3);
    } else {
        n0 = 5; n1 = 4; n2 = 3; n3 = 2;
        conv_task<4>(fp, gp, a0, a1);
        conv_task<2>(fp, gp, a2, a3);
    }
    STAMP(1, 3);
    const float Dv = bflo((unsigned)ssm_D[ch]);
    u16* yb = reinterpret_cast<u16*>(smem + CV_Y);
    const int sl = 16 * (l & 15) + 4 * (l >> 4);
#define EPI(acc, n, i)                                                                        \
    {                                                                                         \
        const int s = 256 * (n) + sl;                                                         \
        const u32x2 uu = *reinterpret_cast<const u32x2*>(base + CV_U + 2 * (2044 - s));       \
        const float u0 = bfhi(uu[1]), u1 = bflo(uu[1]), u2 = bfhi(uu[0]), u3 = bflo(uu[0]);   \
        const unsigned p01 = pk2(rbf(acc[0]) + rbf(u0 * Dv), rbf(acc[1]) + rbf(u1 * Dv));     \
        const unsigned p23 = pk2(rbf(acc[2]) + rbf(u2 * Dv), rbf(acc[3]) + rbf(u3 * Dv));     \
        u16* q = yb + ((i) * 256 + sl) * 4 + w;                                               \
        q[0] = (u16)p01; q[4] = (u16)(p01 >> 16); q[8] = (u16)p23; q[12] = (u16)(p23 >> 16);  \
    }
    EPI(a0, n0, 0) EPI(a1, n1, 1) EPI(a2, n2, 2) EPI(a3, n3, 3)
    __syncthreads();
    {
        const u32x2* yr = reinterpret_cast<const u32x2*>(smem + CV_Y);
        u32x2* dst = reinterpret_cast<u32x2*>(Y4) + (size_t)cq * S_;
        dst[256 * n0 + t] = yr[t];
        dst[256 * n1 + t] = yr[256 + t];
        dst[256 * n2 + t] = yr[512 + t];
        dst[256 * n3 + t] = yr[768 + t];
    }
    STAMP(1, 4);
}

__device__ __forceinline__ float rowsum8(float v) {
    v += __builtin_bit_cast(float, __builtin_amdgcn_update_dpp(0, __builtin_bit_cast(int, v), 0x128, 0xf, 0xf, false));
    {
        const unsigned u = __builtin_bit_cast(unsigned, v);
        auto r = __builtin_amdgcn_permlane16_swap(u, u, false, false);
        v = __builtin_bit_cast(float, (unsigned)r[0]) + __builtin_bit_cast(float, (unsigned)r[1]);
    }
    {
        const unsigned u = __builtin_bit_cast(unsigned, v);
        auto r = __builtin_amdgcn_permlane32_swap(u, u, false, false);
        v = __builtin_bit_cast(float, (unsigned)r[0]) + __builtin_bit_cast(float, (unsigned)r[1]);
    }
    return v;
}
__device__ __forceinline__ f32x2 unpk(unsigned u) { f32x2 r = {bflo(u), bfhi(u)}; return r; }

__device__ __forceinline__ void g1_epilogue(const f32x4 (&ag)[4], const f32x4 (&au)[4], int rb, int ft, int l,
                                            u32x2* __restrict__ hpk, const f32x2* st2, const float* __restrict__ csum) {
    const int ks2 = ft >> 1;
    const int slot = (2 * (ft & 1) + (l >> 5)) * 16 + (l & 15);
    const f32x4 cg = *reinterpret_cast<const f32x4*>(csum + 16 * ft + 4 * (l >> 4));
    const f32x4 cu = *reinterpret_cast<const f32x4*>(csum + F_ + 16 * ft + 4 * (l >> 4));
#pragma unroll
    for (int rt = 0; rt < 4; ++rt) {
        const f32x2 st = st2[16 * rt + (l & 15)];
        float hv[4];
#pragma unroll
        for (int r = 0; r < 4; ++r) {
            const float gv = rbf(ag[rt][r] * st[0] + cg[r] * st[1]);
            const float uv = rbf(au[rt][r] * st[0] + cu[r] * st[1]);
            const float sg = rbf(gv * __builtin_amdgcn_rcpf(1.0f + __expf(-gv)));
            hv[r] = sg * uv;
        }
        u32x2 o;
        o[0] = pk2(hv[0], hv[1]);
        o[1] = pk2(hv[2], hv[3]);
        hpk[((size_t)((rb * 4 + rt) * KS2 + ks2) * 64 + slot) * 2 + ((l >> 4) & 1)] = o;
    }
}

#define G1_LOADW(wbase, ks)                                                                   \
    {                                                                                         \
        wf[ks][0] = gld16<((2 * (ks)) & 3) * 1024>(wbase, wo + ((2 * (ks)) >> 2) * 4096);     \
        wf[ks][1] = gld16<((2 * (ks) + 1) & 3) * 1024>(wbase, wo + ((2 * (ks)) >> 2) * 4096); \
    }
#define G1_STEP(ks, ag, au, nwait, REFILL)                                                    \
    {                                                                                         \
        vm_wait2<nwait>(wf[ks][0], wf[ks][1]);                                                \
        _Pragma("unroll") for (int rt = 0; rt < 4; ++rt) {                                    \
            const u32x4 bb = At[(rt * KS1 + (ks)) * 64 + l];                                  \
            ag[rt] = mfma16(wf[ks][0], bb, ag[rt]);                                           \
            au[rt] = mfma16(wf[ks][1], bb, au[rt]);                                           \
        }                                                                                     \
        if (REFILL) G1_LOADW(wbase2, ks)                                                      \
    }
#define G1_ALLSTEPS(ag, au, W0, DW, REFILL)                                                                       \
    G1_STEP(0, ag, au, W0, REFILL) G1_STEP(1, ag, au, W0 - DW, REFILL) G1_STEP(2, ag, au, W0 - 2 * DW, REFILL)    \
    G1_STEP(3, ag, au, W0 - 3 * DW, REFILL) G1_STEP(4, ag, au, W0 - 4 * DW, REFILL)                               \
    G1_STEP(5, ag, au, W0 - 5 * DW, REFILL) G1_STEP(6, ag, au, W0 - 6 * DW, REFILL)                               \
    G1_STEP(7, ag, au, W0 - 7 * DW, REFILL) G1_STEP(8, ag, au, W0 - 8 * DW, REFILL)                               \
    G1_STEP(9, ag, au, W0 - 9 * DW, REFILL) G1_STEP(10, ag, au, W0 - 10 * DW, REFILL)                             \
    G1_STEP(11, ag, au, W0 - 11 * DW, REFILL) G1_STEP(12, ag, au, W0 - 12 * DW, REFILL)                           \
    G1_STEP(13, ag, au, W0 - 13 * DW, REFILL) G1_STEP(14, ag, au, W0 - 14 * DW, REFILL)                           \
    G1_STEP(15, ag, au, W0 - 15 * DW, REFILL)

template <bool TWO>
__device__ __forceinline__ void g1_main(u32x4 (&wf)[KS1][2], const u32x4* At, const u32x4* wbase2, unsigned wo, int l,
                                        int rb, int ft, u32x2* __restrict__ hpk, const f32x2* st2,
                                        const float* __restrict__ csum) {
    f32x4 ag[4], au[4];
#pragma unroll
    for (int r = 0; r < 4; ++r) { ag[r] = (f32x4){0.f, 0.f, 0.f, 0.f}; au[r] = ag[r]; }
    if (TWO) {
        G1_ALLSTEPS(ag, au, 30, 0, true)
        f32x4 bg[4], bu[4];
#pragma unroll
        for (int r = 0; r < 4; ++r) { bg[r] = (f32x4){0.f, 0.f, 0.f, 0.f}; bu[r] = bg[r]; }
        G1_ALLSTEPS(bg, bu, 30, 2, false)
        STAMP(2, 6);
        g1_epilogue(ag, au, rb, ft, l, hpk, st2, csum);
        g1_epilogue(bg, bu, rb, ft + 8, l, hpk, st2, csum);
    } else {
        G1_ALLSTEPS(ag, au, 30, 2, false)
        STAMP(2, 6);
        g1_epilogue(ag, au, rb, ft, l, hpk, st2, csum);
    }
}

__global__ __launch_bounds__(512, 2) void g1_kernel(const u16* __restrict__ x, const f32x2* __restrict__ part,
                                                    const u32x2* __restrict__ Y4, const u32x4* __restrict__ Wgu,
                                                    const float* __restrict__ csum, u16* __restrict__ x1out,
                                                    u32x2* __restrict__ hpk) {
    __shared__ u32x4 At[4 * KS1 * 64 + 32];
    f32x2* st2 = reinterpret_cast<f32x2*>(At + 4 * KS1 * 64);
    const int b = blockIdx.x;
    const int rb = (b & 7) * 4 + (b >> 6), cb = (b >> 3) & 7;
    const int l = threadIdx.x & 63;
    const int w = __builtin_amdgcn_readfirstlane(threadIdx.x >> 6);
    const int ntl = cb < 6 ? 11 : 10;
    const int ft = (cb < 6 ? 11 * cb : 66 + 10 * (cb - 6)) + w;
    const bool two = w + 8 < ntl;
    const u32x4* wbase = Wgu + (size_t)ft * (KS1 * 2 * 64);
    const u32x4* wbase2 = wbase + (size_t)(two ? 8 : 0) * (KS1 * 2 * 64);
    const unsigned wo = 16 * l;
    u32x4 wf[KS1][2];
    STAMP(2, 0);
    {
        const int i = l & 7, g = l >> 3;
        const int row = rb * 64 + 8 * w + i;
        u32x4 xv[8];
        u32x2 yq[16];
        u32x2 pt = gld8<0>(part, (unsigned)(row * 8 + g) * 8);
        const unsigned xo = row * 1024 + 16 * g;
        xv[0] = gld16<0>(x, xo);   xv[1] = gld16<128>(x, xo); xv[2] = gld16<256>(x, xo); xv[3] = gld16<384>(x, xo);
        xv[4] = gld16<512>(x, xo); xv[5] = gld16<640>(x, xo); xv[6] = gld16<768>(x, xo); xv[7] = gld16<896>(x, xo);
#pragma unroll
        for (int c = 0; c < 8; ++c) {
            const unsigned yo = (16 * c + 2 * g) * 16384 + row * 8;
            yq[2 * c] = gld8<0>(Y4, yo);
            yq[2 * c + 1] = gld8<0>(Y4, yo + 16384);
        }
        __builtin_amdgcn_s_barrier();
        G1_LOADW(wbase, 0) G1_LOADW(wbase, 1) G1_LOADW(wbase, 2) G1_LOADW(wbase, 3)
        vm_wait1<32>(pt);
        const float mean = rowsum8(__builtin_bit_cast(float, (unsigned)pt[0])) * (1.0f / 512.0f);
        const float msq = rowsum8(__builtin_bit_cast(float, (unsigned)pt[1])) * (1.0f / 512.0f);
        const float inv = 1.0f / sqrtf(fmaxf(msq - mean * mean, 0.f) + 1e-5f);
        vm_wait8<24>(xv);
        STAMP(2, 1);
        vm_wait16<8>(yq);
        STAMP(2, 3);
        const int rt = w >> 1, s16 = 8 * (w & 1) + i;
        const f32x2 i2 = {inv, inv};
        const f32x2 b2 = {-mean * inv, -mean * inv};
        f32x2 sa = {0.f, 0.f}, sb = sa, qa = sa, qb = sa;
#define G1_TRANS(c)                                                                                       \
        {                                                                                                 \
            u32x4 o;                                                                                      \
            _Pragma("unroll") for (int j = 0; j < 4; ++j) {                                               \
                const f32x2 xx = unpk(xv[c][j]);                                                          \
                const f32x2 yy = unpk(yq[2 * (c) + (j >> 1)][j & 1]);                                     \
                const f32x2 q = xx * i2 + b2;                                                             \
                const f32x2 r = xx + q * yy;                                                              \
                o[j] = pk2(r[0], r[1]);                                                                   \
                if (j & 1) { sb += r; qb += r * r; } else { sa += r; qa += r * r; }                       \
            }                                                                                             \
            At[(rt * KS1 + 2 * (c) + (g >> 2)) * 64 + (g & 3) * 16 + s16] = o;                            \
            if ((c) == cb) *reinterpret_cast<u32x4*>(x1out + (size_t)row * D_ + 64 * (c) + 8 * g) = o;    \
            __builtin_amdgcn_sched_barrier(0);                                                            \
        }
        G1_TRANS(0) G1_LOADW(wbase, 4)
        G1_TRANS(1) G1_LOADW(wbase, 5)
        G1_TRANS(2) G1_LOADW(wbase, 6)
        G1_TRANS(3) G1_LOADW(wbase, 7)
        G1_TRANS(4) G1_LOADW(wbase, 8)
        G1_TRANS(5) G1_LOADW(wbase, 9)
        G1_TRANS(6) G1_LOADW(wbase, 10)
        G1_TRANS(7) G1_LOADW(wbase, 11)
        sa += sb; qa += qb;
        const float mean2 = rowsum8(sa[0] + sa[1]) * (1.0f / 512.0f);
        const float msq2 = rowsum8(qa[0] + qa[1]) * (1.0f / 512.0f);
        const float inv2 = 1.0f / sqrtf(fmaxf(msq2 - mean2 * mean2, 0.f) + 1e-5f);
        if (g == 0) { f32x2 o = {inv2, -mean2 * inv2}; st2[8 * w + i] = o; }
        G1_LOADW(wbase, 12) G1_LOADW(wbase, 13) G1_LOADW(wbase, 14) G1_LOADW(wbase, 15)
    }
    STAMP(2, 4);
    __syncthreads();
    STAMP(2, 5);
    if (two) g1_main<true>(wf, At, wbase2, wo, l, rb, ft, hpk, st2, csum);
    else g1_main<false>(wf, At, wbase2, wo, l, rb, ft, hpk, st2, csum);
    STAMP(2, 7);
}

template <bool SIX>
__device__ __forceinline__ void g2_main(const u32x4* hbase, const u32x4* wbase, int l, f32x4 (&acc)[4][4]) {
    u32x4 af[5][4], bf[5][4];
    unsigned vo[4];
#pragma unroll
    for (int i = 0; i < 4; ++i) vo[i] = 16 * l + i * (KS2 * 1024);
#define G2_LOAD(st)                                                                                   \
    _Pragma("unroll") for (int i = 0; i < 4; ++i) {                                                   \
        af[(st) % 5][i] = gld16<((st) & 3) * 1024>(wbase, vo[i] + ((st) >> 2) * 4096);                \
        bf[(st) % 5][i] = gld16<((st) & 3) * 1024>(hbase, vo[i] + ((st) >> 2) * 4096);                \
    }
#define G2_STEP(st, nwait)                                                                            \
    {                                                                                                 \
        vm_wait8<nwait>(af[(st) % 5], bf[(st) % 5]);                                                  \
        _Pragma("unroll") for (int i = 0; i < 4; ++i)                                                 \
            _Pragma("unroll") for (int j = 0; j < 4; ++j)                                             \
                acc[i][j] = mfma16(af[(st) % 5][i], bf[(st) % 5][j], acc[i][j]);                      \
    }
    G2_LOAD(0) G2_LOAD(1) G2_LOAD(2) G2_LOAD(3) G2_LOAD(4)
    STAMP(3, 1);
    G2_STEP(0, 32)
    STAMP(3, 2);
    if (SIX) {
        G2_LOAD(5)
        G2_STEP(1, 32) G2_STEP(2, 24) G2_STEP(3, 16) G2_STEP(4, 8) G2_STEP(5, 0)
    } else {
        G2_STEP(1, 24) G2_STEP(2, 16) G2_STEP(3, 8) G2_STEP(4, 0)
    }
}

__device__ __forceinline__ float quadsum(float v) {
    {
        const unsigned u = __builtin_bit_cast(unsigned, v);
        auto r = __builtin_amdgcn_permlane16_swap(u, u, false, false);
        v = __builtin_bit_cast(float, (unsigned)r[0]) + __builtin_bit_cast(float, (unsigned)r[1]);
    }
    {
        const unsigned u = __builtin_bit_cast(unsigned, v);
        auto r = __builtin_amdgcn_permlane32_swap(u, u, false, false);
        v = __builtin_bit_cast(float, (unsigned)r[0]) + __builtin_bit_cast(float, (unsigned)r[1]);
    }
    return v;
}

template <bool STATS>
__global__ __launch_bounds__(512, 2) void g2_kernel(const u32x4* __restrict__ hpk, const u32x4* __restrict__ Wd,
                                                    const u16* __restrict__ xin, u16* __restrict__ xout,
                                                    f32x2* __restrict__ part) {
    __shared__ f32x4 red[8 * 16 * 64 + 128];
    f32x2* sred = reinterpret_cast<f32x2*>(red + 8 * 16 * 64);
    const int b = blockIdx.x;
    const int rb = (b & 7) * 4 + (b >> 6), cb = (b >> 3) & 7;
    const int l = threadIdx.x & 63;
    const int w = __builtin_amdgcn_readfirstlane(threadIdx.x >> 6);
    const int start = 5 * w + (w < 3 ? w : 3);
    const u32x4* hbase = hpk + ((size_t)(rb * 4) * KS2 + start) * 64;
    const u32x4* wbase = Wd + ((size_t)(cb * 4) * KS2 + start) * 64;
    const int n = cb * 64 + 16 * (w >> 1) + 4 * (l >> 4);
    const int s0 = rb * 64 + 32 * (w & 1) + (l & 15);
    const unsigned xo = (unsigned)(s0 * D_ + n) * 2;
    STAMP(3, 0);
    u32x2 xr0 = gld8<0>(xin, xo), xr1 = gld8<0>(xin, xo + 16 * D_ * 2);
    f32x4 acc[4][4];
#pragma unroll
    for (int i = 0; i < 4; ++i)
#pragma unroll
        for (int j = 0; j < 4; ++j) acc[i][j] = (f32x4){0.f, 0.f, 0.f, 0.f};
    if (w < 3) g2_main<true>(hbase, wbase, l, acc);
    else g2_main<false>(hbase, wbase, l, acc);
    STAMP(3, 3);
#pragma unroll
    for (int i = 0; i < 4; ++i)
#pragma unroll
        for (int j = 0; j < 4; ++j) red[(w * 16 + i * 4 + j) * 64 + l] = acc[i][j];
    STAMP(3, 4);
    __syncthreads();
    STAMP(3, 5);
    vm_wait2<0>(xr0, xr1);
#pragma unroll
    for (int q = 0; q < 2; ++q) {
        const int tile = 2 * w + q;
        const u32x2 xr = q ? xr1 : xr0;
        f32x4 sum = red[tile * 64 + l];
#pragma unroll
        for (int p = 1; p < 8; ++p) sum += red[(p * 16 + tile) * 64 + l];
        u32x2 o;
        o[0] = pk2(bflo(xr[0]) + rbf(sum[0]), bfhi(xr[0]) + rbf(sum[1]));
        o[1] = pk2(bflo(xr[1]) + rbf(sum[2]), bfhi(xr[1]) + rbf(sum[3]));
        *reinterpret_cast<u32x2*>(xout + (size_t)(s0 + 16 * q) * D_ + n) = o;
        if (STATS) {
            const float v0 = bflo(o[0]), v1 = bfhi(o[0]), v2 = bflo(o[1]), v3 = bfhi(o[1]);
            const float sm = quadsum((v0 + v1) + (v2 + v3));
            const float sq = quadsum((v0 * v0 + v1 * v1) + (v2 * v2 + v3 * v3));
            if (l < 16) { f32x2 e = {sm, sq}; sred[(w >> 1) * 64 + 32 * (w & 1) + 16 * q + l] = e; }
        }
    }
    if (STATS) {
        __syncthreads();
        if (threadIdx.x < 64) {
            const f32x2 e = (sred[threadIdx.x] + sred[64 + threadIdx.x]) + (sred[128 + threadIdx.x] + sred[192 + threadIdx.x]);
            part[(size_t)(rb * 64 + threadIdx.x) * 8 + cb] = e;
        }
    }
    STAMP(3, 6);
}

extern "C" void kernel_launch(void* const* d_in, const int* in_sizes, int n_in,
                              void* d_out, int out_size, void* d_ws, size_t ws_size,
                              hipStream_t stream) {
    const u16* q       = (const u16*)d_in[0];
    const u16* k       = (const u16*)d_in[1];
    const u16* v       = (const u16*)d_in[2];
    const u16* shift_k = (const u16*)d_in[3];
    const u16* ssm_k   = (const u16*)d_in[4];
    const u16* ssm_D   = (const u16*)d_in[5];
    const u16* w_gate  = (const u16*)d_in[7];
    const u16* w_up    = (const u16*)d_in[8];
    const u16* w_down  = (const u16*)d_in[9];
    u16* out = (u16*)d_out;

    char* ws = (char*)d_ws;
    const size_t MB2 = (size_t)S_ * D_ * 2;
    u16*   kvR = (u16*)(ws);
    u16*   Y4  = (u16*)(ws + MB2);
    u16*   x1  = (u16*)(ws + 2 * MB2);
    u16*   x2  = (u16*)(ws + 3 * MB2);
    u16*   x3  = (u16*)(ws + 4 * MB2);
    u32x4* Wgu = (u32x4*)(ws + 5 * MB2);
    u32x4* Wd  = (u32x4*)(ws + 5 * MB2 + (size_t)NFT * KS1 * 2 * 1024);
    u32x4* hpk = (u32x4*)(ws + 5 * MB2 + (size_t)NFT * KS1 * 2 * 1024 + (size_t)32 * KS2 * 1024);

    char* tail = (char*)hpk + (size_t)128 * KS2 * 1024;
    float* csum  = (float*)tail;
    f32x2* part0 = (f32x2*)(tail + 16384);
    f32x2* part2 = (f32x2*)(tail + 16384 + 131072);

    prep_kernel<<<256 + PACK_BLOCKS + 86 + 256, 256, 0, stream>>>(k, v, shift_k, w_gate, w_up, w_down, q, kvR, Wgu, Wd,
                                                                  csum, part0);
    conv_kernel<<<256, 256, 0, stream>>>(kvR, ssm_k, ssm_D, Y4);
    g1_kernel<<<256, 512, 0, stream>>>(q, part0, (const u32x2*)Y4, Wgu, csum, x1, (u32x2*)hpk);
    g2_kernel<true><<<256, 512, 0, stream>>>(hpk, Wd, x1, x2, part2);
    g1_kernel<<<256, 512, 0, stream>>>(x2, part2, (const u32x2*)Y4, Wgu, csum, x3, (u32x2*)hpk);
    g2_kernel<false><<<256, 512, 0, stream>>>(hpk, Wd, x3, out, part2);
}
```

```cpp
#include <hip/hip_runtime.h>
#include <hip/hip_bf16.h>
#include <stdint.h>

typedef __attribute__((ext_vector_type(8))) __bf16   bf16x8;
typedef __attribute__((ext_vector_type(2))) __bf16   bf16x2;
typedef __attribute__((ext_vector_type(4))) float    f32x4;
typedef __attribute__((ext_vector_type(2))) float    f32x2;
typedef __attribute__((ext_vector_type(4))) unsigned u32x4;
typedef __attribute__((ext_vector_type(2))) unsigned u32x2;
typedef unsigned short u16;

#define STAMP(k, i)

constexpr int S_ = 2048, D_ = 512, F_ = 1376;
constexpr int NFT = 86;
constexpr int KS1 = 16;
constexpr int KS2 = 43;

__device__ __forceinline__ float bflo(unsigned u) { return __builtin_bit_cast(float, u << 16); }
__device__ __forceinline__ float bfhi(unsigned u) { return __builtin_bit_cast(float, u & 0xffff0000u); }
__device__ __forceinline__ unsigned pk2(float lo, float hi) {
    f32x2 v = {lo, hi};
    bf16x2 r = __builtin_convertvector(v, bf16x2);
    return __builtin_bit_cast(unsigned, r);
}
__device__ __forceinline__ float rbf(float x) { return bflo(pk2(x, x)); }
__device__ __forceinline__ f32x4 mfma16(u32x4 a, u32x4 b, f32x4 c) {
    return __builtin_amdgcn_mfma_f32_16x16x32_bf16(__builtin_bit_cast(bf16x8, a), __builtin_bit_cast(bf16x8, b), c, 0, 0, 0);
}

template <int IMM> __device__ __forceinline__ u32x4 gld16(const void* sbase, unsigned voff) {
    u32x4 r;
    asm volatile("global_load_dwordx4 %0, %1, %2 offset:%3" : "=v"(r) : "v"(voff), "s"(sbase), "i"(IMM) : "memory");
    return r;
}
template <int IMM> __device__ __forceinline__ u32x2 gld8(const void* sbase, unsigned voff) {
    u32x2 r;
    asm volatile("global_load_dwordx2 %0, %1, %2 offset:%3" : "=v"(r) : "v"(voff), "s"(sbase), "i"(IMM) : "memory");
    return r;
}
template <int N> __device__ __forceinline__ void vm_wait2(u32x4& a, u32x4& b) {
    asm volatile("s_waitcnt vmcnt(%2)" : "+v"(a), "+v"(b) : "i"(N) : "memory");
}
template <int N> __device__ __forceinline__ void vm_wait8(u32x4 (&a)[4], u32x4 (&b)[4]) {
    asm volatile("s_waitcnt vmcnt(%8)"
                 : "+v"(a[0]), "+v"(a[1]), "+v"(a[2]), "+v"(a[3]), "+v"(b[0]), "+v"(b[1]), "+v"(b[2]), "+v"(b[3])
                 : "i"(N) : "memory");
}
template <int N> __device__ __forceinline__ void vm_wait8(u32x4 (&a)[8]) {
    asm volatile("s_waitcnt vmcnt(%8)"
                 : "+v"(a[0]), "+v"(a[1]), "+v"(a[2]), "+v"(a[3]), "+v"(a[4]), "+v"(a[5]), "+v"(a[6]), "+v"(a[7])
                 : "i"(N) : "memory");
}
template <int N> __device__ __forceinline__ void vm_wait16(u32x2 (&a)[16]) {
    asm volatile("s_waitcnt vmcnt(%16)"
                 : "+v"(a[0]), "+v"(a[1]), "+v"(a[2]), "+v"(a[3]), "+v"(a[4]), "+v"(a[5]), "+v"(a[6]), "+v"(a[7]),
                   "+v"(a[8]), "+v"(a[9]), "+v"(a[10]), "+v"(a[11]), "+v"(a[12]), "+v"(a[13]), "+v"(a[14]), "+v"(a[15])
                 : "i"(N) : "memory");
}
template <int N> __device__ __forceinline__ void vm_wait1(u32x2& a) {
    asm volatile("s_waitcnt vmcnt(%1)" : "+v"(a) : "i"(N) : "memory");
}
template <int N> __device__ __forceinline__ void vm_wait2(u32x2& a, u32x2& b) {
    asm volatile("s_waitcnt vmcnt(%2)" : "+v"(a), "+v"(b) : "i"(N) : "memory");
}

constexpr int PACK_GU_THREADS = 2 * 64 * 688;
constexpr int PACK_D_THREADS  = 172 * 256;
constexpr int PACK_BLOCKS = (PACK_GU_THREADS + PACK_D_THREADS) / 256;

__device__ __forceinline__ void pack_item(const u16* __restrict__ src, int N, int ko, int np, u32x4* __restrict__ dst0) {
    unsigned w[8];
#pragma unroll
    for (int j = 0; j < 8; ++j) w[j] = *reinterpret_cast<const unsigned*>(src + (size_t)(8 * ko + j) * N + 2 * np);
    u32x4 o0, o1;
#pragma unroll
    for (int c = 0; c < 4; ++c) {
        o0[c] = (w[2 * c] & 0xffffu) | (w[2 * c + 1] << 16);
        o1[c] = (w[2 * c] >> 16) | (w[2 * c + 1] & 0xffff0000u);
    }
    dst0[0] = o0;
    dst0[1] = o1;
}

__global__ __launch_bounds__(256) void prep_kernel(const u16* __restrict__ k, const u16* __restrict__ v,
                                                   const u16* __restrict__ shift_k,
                                                   const u16* __restrict__ wg, const u16* __restrict__ wu,
                                                   const u16* __restrict__ wd,
                                                   const u16* __restrict__ x0,
                                                   u16* __restrict__ kvR, u32x4* __restrict__ Wgu,
                                                   u32x4* __restrict__ Wd, float* __restrict__ csum,
                                                   f32x2* __restrict__ part0, u32x2* __restrict__ x0t) {
    __shared__ unsigned tile[64 * 33];
    const int t = threadIdx.x;
    STAMP(0, 0);
    if (blockIdx.x < 256) {
        const int st = blockIdx.x >> 3, dt = blockIdx.x & 7;
        const int s0 = st * 64, d0 = dt * 64;
        const int c8 = t & 7, r = t >> 3;
        const int sA = s0 + 2 * r;
        const int dd = d0 + 8 * c8;
        u32x4 kr[5], vr[2], wv[4];
#pragma unroll
        for (int i = 0; i < 5; ++i) {
            const int s = sA - 3 + i;
            u32x4 z = {0u, 0u, 0u, 0u};
            kr[i] = (s >= 0) ? *reinterpret_cast<const u32x4*>(k + (size_t)s * D_ + dd) : z;
        }
#pragma unroll
        for (int i = 0; i < 2; ++i) vr[i] = *reinterpret_cast<const u32x4*>(v + (size_t)(sA + i) * D_ + dd);
#pragma unroll
        for (int i = 0; i < 4; ++i) wv[i] = *reinterpret_cast<const u32x4*>(shift_k + (size_t)dd * 4 + 8 * i);
#pragma unroll
        for (int j = 0; j < 8; ++j) {
            const unsigned wa = wv[j >> 1][(j & 1) * 2], wb = wv[j >> 1][(j & 1) * 2 + 1];
            const float w0 = bflo(wa), w1 = bfhi(wa), w2 = bflo(wb), w3 = bfhi(wb);
            float kk[5];
#pragma unroll
            for (int i = 0; i < 5; ++i) kk[i] = (j & 1) ? bfhi(kr[i][j >> 1]) : bflo(kr[i][j >> 1]);
            float res[2];
#pragma unroll
            for (int q = 0; q < 2; ++q) {
                float acc = rbf(kk[q] * w3);
                acc = rbf(acc + rbf(kk[q + 1] * w2));
                acc = rbf(acc + rbf(kk[q + 2] * w1));
                acc = rbf(acc + rbf(kk[q + 3] * w0));
                const float vv = (j & 1) ? bfhi(vr[q][j >> 1]) : bflo(vr[q][j >> 1]);
                res[q] = acc * vv;
            }
            tile[(8 * c8 + j) * 33 + (31 - r)] = pk2(res[1], res[0]);
        }
        STAMP(0, 1);
        __syncthreads();
#pragma unroll
        for (int i = 0; i < 2; ++i) {
            const int idx = t + 256 * i;
            const int d = idx >> 3, q = idx & 7;
            u32x4 o;
#pragma unroll
            for (int c = 0; c < 4; ++c) o[c] = tile[d * 33 + 4 * q + c];
            *reinterpret_cast<u32x4*>(kvR + (size_t)(d0 + d) * S_ + (S_ - 64 - s0) + 8 * q) = o;
        }
    } else if (blockIdx.x >= 256 + PACK_BLOCKS + 86) {
        const int r0 = (blockIdx.x - (256 + PACK_BLOCKS + 86)) * 16;
        const int row = t & 15;
        f32x2* sp = reinterpret_cast<f32x2*>(tile);
        u32x4 a[4];
#pragma unroll
        for (int i = 0; i < 4; ++i) a[i] = *reinterpret_cast<const u32x4*>(x0 + (size_t)(r0 + row) * D_ + 8 * ((t >> 4) + 16 * i));
#pragma unroll
        for (int i = 0; i < 4; ++i) {
            const int o = (t >> 4) + 16 * i;
            u32x2 lo = {a[i][0], a[i][1]}, hi = {a[i][2], a[i][3]};
            x0t[(size_t)(2 * o) * S_ + r0 + row] = lo;
            x0t[(size_t)(2 * o + 1) * S_ + r0 + row] = hi;
            float sm = 0.f, sq = 0.f;
#pragma unroll
            for (int j = 0; j < 4; ++j) {
                const float l0 = bflo(a[i][j]), h0 = bfhi(a[i][j]);
                sm += l0 + h0;
                sq += l0 * l0 + h0 * h0;
            }
            f32x2 e = {sm, sq};
            sp[row * 64 + o] = e;
        }
        __syncthreads();
        if (t < 128) {
            const int rr = t >> 3, g = t & 7;
            f32x2 e = sp[rr * 64 + 8 * g];
#pragma unroll
            for (int j = 1; j < 8; ++j) e += sp[rr * 64 + 8 * g + j];
            part0[(size_t)(r0 + rr) * 8 + g] = e;
        }
    } else if (blockIdx.x >= 256 + PACK_BLOCKS) {
        const int cbk = blockIdx.x - (256 + PACK_BLOCKS);
        const int m = cbk / 43, f0 = (cbk % 43) * 32;
        const u16* src = m ? wu : wg;
        const int np = t & 15, kg = t >> 4;
        unsigned wv[32];
#pragma unroll
        for (int j = 0; j < 32; ++j) wv[j] = *reinterpret_cast<const unsigned*>(src + (size_t)(32 * kg + j) * F_ + f0 + 2 * np);
        float s0 = 0.f, s1 = 0.f;
#pragma unroll
        for (int j = 0; j < 32; ++j) { s0 += bflo(wv[j]); s1 += bfhi(wv[j]); }
        float* red = reinterpret_cast<float*>(tile);
        red[kg * 33 + 2 * np] = s0;
        red[kg * 33 + 2 * np + 1] = s1;
        __syncthreads();
        if (t < 32) {
            float a = 0.f;
#pragma unroll
            for (int j = 0; j < 16; ++j) a += red[j * 33 + t];
            csum[m * F_ + f0 + t] = a;
        }
    } else {
        int id = (blockIdx.x - 256) * 256 + t;
        if (id < PACK_GU_THREADS) {
            const int np = id % 688;
            const int rest = id / 688;
            const int ko = rest & 63, m = rest >> 6;
            const int n = 2 * np;
            const int ft = n >> 4, ks = ko >> 2, g = ko & 3;
            pack_item(m ? wu : wg, F_, ko, np, Wgu + ((size_t)((ft * KS1 + ks) * 2 + m) * 64 + g * 16 + (n & 15)));
        } else {
            id -= PACK_GU_THREADS;
            const int np = id & 255;
            const int ko = id >> 8;
            const int n = 2 * np;
            const int nt = n >> 4, ks = ko >> 2, g = ko & 3;
            pack_item(wd, D_, ko, np, Wd + ((size_t)(nt * KS2 + ks) * 64 + g * 16 + (n & 15)));
        }
    }
    STAMP(0, 2);
}

constexpr int CV_C = 4160;
constexpr int CV_U = 4 * CV_C;
constexpr int CV_CH = CV_U + 4608;
constexpr int CV_Y = 4 * CV_CH;

template <int OFF> __device__ __forceinline__ u32x2 lds8(unsigned addr) {
    u32x2 r;
    asm volatile("ds_read_b64 %0, %1 offset:%2" : "=v"(r) : "v"(addr), "i"(OFF) : "memory");
    return r;
}
template <int OFF> __device__ __forceinline__ u32x4 lds16(unsigned addr) {
    u32x4 r;
    asm volatile("ds_read_b128 %0, %1 offset:%2" : "=v"(r) : "v"(addr), "i"(OFF) : "memory");
    return r;
}
__device__ __forceinline__ void lgkm_wait(u32x2 (&a)[16], u32x4 (&b)[8]) {
    asm volatile("s_waitcnt lgkmcnt(0)"
                 : "+v"(a[0]), "+v"(a[1]), "+v"(a[2]), "+v"(a[3]), "+v"(a[4]), "+v"(a[5]), "+v"(a[6]), "+v"(a[7]),
                   "+v"(a[8]), "+v"(a[9]), "+v"(a[10]), "+v"(a[11]), "+v"(a[12]), "+v"(a[13]), "+v"(a[14]), "+v"(a[15]),
                   "+v"(b[0]), "+v"(b[1]), "+v"(b[2]), "+v"(b[3]), "+v"(b[4]), "+v"(b[5]), "+v"(b[6]), "+v"(b[7])
                 :: "memory");
}
template <int BLK, int GBLK>
__device__ __forceinline__ void conv_load(unsigned fa, unsigned ga, u32x2 (&F)[16], u32x4 (&G)[8]) {
#define CVL(i) F[2 * (i)] = lds8<64 * (8 * BLK + (i))>(fa); F[2 * (i) + 1] = lds8<64 * (8 * BLK + (i)) + 8>(fa); \
               G[i] = lds16<64 * (8 * (GBLK + 7) + (i))>(ga);
    CVL(0) CVL(1) CVL(2) CVL(3) CVL(4) CVL(5) CVL(6) CVL(7)
#undef CVL
}
__device__ __forceinline__ u32x4 cat(u32x2 a, u32x2 b) { u32x4 r = {a[0], a[1], b[0], b[1]}; return r; }

template <int N, int T>
__device__ __forceinline__ void conv_step(unsigned fa, unsigned ga, u32x2 (&Fr)[3][16], u32x4 (&Gr)[2][8], f32x4& acc1,
                                          f32x4& acc0) {
    if (T >= 0) lgkm_wait(Fr[T % 3], Gr[T & 1]);
    if (T + 1 <= N + 1) conv_load<T + 1, T - N>(fa, ga, Fr[(T + 1) % 3], Gr[(T + 1) & 1]);
    __builtin_amdgcn_sched_barrier(0);
    if (T >= 0) {
#pragma unroll
        for (int i = 0; i < 8; ++i) {
            acc1 = mfma16(cat(Fr[T % 3][2 * i], Fr[T % 3][2 * i + 1]), Gr[T & 1][i], acc1);
            if (T >= 1) acc0 = mfma16(cat(Fr[(T + 2) % 3][2 * i], Fr[(T + 2) % 3][2 * i + 1]), Gr[T & 1][i], acc0);
        }
    }
    __builtin_amdgcn_sched_barrier(0);
    if constexpr (T < N + 1) conv_step<N, T + 1>(fa, ga, Fr, Gr, acc1, acc0);
}
template <int N>
__device__ __forceinline__ void conv_task(unsigned fa, unsigned ga, f32x4& acc1, f32x4& acc0) {
    u32x2 Fr[3][16];
    u32x4 Gr[2][8];
    conv_step<N, -1>(fa, ga, Fr, Gr, acc1, acc0);
}

__device__ __forceinline__ unsigned sh16(unsigned lo, unsigned hi) { return (lo >> 16) | (hi << 16); }

__global__ __launch_bounds__(256) void conv_kernel(const u16* __restrict__ kvR, const u16* __restrict__ ssm_k,
                                                   const u16* __restrict__ ssm_D, u16* __restrict__ Y4) {
    __shared__ __attribute__((aligned(16))) char smem[CV_Y + 8704];
    const int t = threadIdx.x, l = t & 63;
    const int w = __builtin_amdgcn_readfirstlane(t >> 6);
    const int cq = blockIdx.x >> 1, half = blockIdx.x & 1;
    const int ch = 4 * cq + w;
    char* base = smem + w * CV_CH;
    STAMP(1, 0);
    const float Dv = bflo((unsigned)ssm_D[ch]);
    {
        const u16* hp = ssm_k + (size_t)ch * S_;
        const u16* up = kvR + (size_t)ch * S_;
        u32x4 hv[4], uv[4];
        u32x2 hx[4];
#pragma unroll
        for (int i = 0; i < 4; ++i) {
            const int m = l + 64 * i;
            hv[i] = *reinterpret_cast<const u32x4*>(hp + 8 * m);
            uv[i] = *reinterpret_cast<const u32x4*>(up + 8 * m);
            const u32x2 zz = {0u, 0u};
            hx[i] = (m < 255) ? *reinterpret_cast<const u32x2*>(hp + 8 * m + 8) : zz;
        }
        const u32x4 z = {0u, 0u, 0u, 0u};
        if (l < 32) *reinterpret_cast<u32x4*>(base + CV_U + 4096 + 16 * l) = z;
        if (l < 4) *reinterpret_cast<u32x4*>(base + l * CV_C) = z;
        if (l == 0) {
            const unsigned h01 = hv[0][0], h23 = hv[0][1];
            u32x4 c0 = z, c1 = z, c2 = z, c3 = z;
            c0[3] = h01 << 16;
            c1[3] = h01;
            c2[2] = h01 << 16; c2[3] = sh16(h01, h23);
            c3[2] = h01;       c3[3] = h23;
            *reinterpret_cast<u32x4*>(base + 0 * CV_C + 16) = c0;
            *reinterpret_cast<u32x4*>(base + 1 * CV_C + 16) = c1;
            *reinterpret_cast<u32x4*>(base + 2 * CV_C + 16) = c2;
            *reinterpret_cast<u32x4*>(base + 3 * CV_C + 16) = c3;
        }
#pragma unroll
        for (int i = 0; i < 4; ++i) {
            const int m = l + 64 * i;
            const unsigned d0 = hv[i][0], d1 = hv[i][1], d2 = hv[i][2], d3 = hv[i][3], d4 = hx[i][0], d5 = hx[i][1];
            const unsigned e0 = sh16(d0, d1), e1 = sh16(d1, d2), e2 = sh16(d2, d3), e3 = sh16(d3, d4), e4 = sh16(d4, d5);
            u32x4 c0 = {e0, e1, e2, e3}, c1 = {d1, d2, d3, d4}, c2 = {e1, e2, e3, e4}, c3 = {d2, d3, d4, d5};
            *reinterpret_cast<u32x4*>(base + 0 * CV_C + 32 + 16 * m) = c0;
            *reinterpret_cast<u32x4*>(base + 1 * CV_C + 32 + 16 * m) = c1;
            *reinterpret_cast<u32x4*>(base + 2 * CV_C + 32 + 16 * m) = c2;
            *reinterpret_cast<u32x4*>(base + 3 * CV_C + 32 + 16 * m) = c3;
            *reinterpret_cast<u32x4*>(base + CV_U + 16 * m) = uv[i];
        }
    }
    STAMP(1, 1);
    __syncthreads();
    STAMP(1, 2);

    const int o0 = (l & 15) + 8 * (l >> 4);
    const unsigned fp = (unsigned)(size_t)(base + (l & 3) * CV_C + 2 * (o0 & ~3));
    const unsigned gp = (unsigned)(size_t)(base + CV_U + 2 * (2032 - 16 * (l & 15) + 8 * (l >> 4)) - 64 * 56);
    f32x4 a0 = {0.f, 0.f, 0.f, 0.f}, a1 = a0, a2 = a0, a3 = a0;
    int n0, n1, n2, n3;
    if (half == 0) {
        n0 = 7; n1 = 6; n2 = 1; n3 = 0;
        conv_task<6>(fp, gp, a0, a1);
        conv_task<0>(fp, gp, a2, a3);
    } else {
        n0 = 5; n1 = 4; n2 = 3; n3 = 2;
        conv_task<4>(fp, gp, a0, a1);
        conv_task<2>(fp, gp, a2, a3);
    }
    STAMP(1, 3);
    u16* yb = reinterpret_cast<u16*>(smem + CV_Y);
    const int sl = 16 * (l & 15) + 4 * (l >> 4);
    const int slp = sl + (l & 15);
#define EPI(acc, n, i)                                                                        \
    {                                                                                         \
        const int s = 256 * (n) + sl;                                                         \
        const u32x2 uu = *reinterpret_cast<const u32x2*>(base + CV_U + 2 * (2044 - s));       \
        const float u0 = bfhi(uu[1]), u1 = bflo(uu[1]), u2 = bfhi(uu[0]), u3 = bflo(uu[0]);   \
        const unsigned c01 = pk2(acc[0], acc[1]), c23 = pk2(acc[2], acc[3]);                  \
        const unsigned k01 = pk2(u0 * Dv, u1 * Dv), k23 = pk2(u2 * Dv, u3 * Dv);              \
        const unsigned p01 = pk2(bflo(c01) + bflo(k01), bfhi(c01) + bfhi(k01));               \
        const unsigned p23 = pk2(bflo(c23) + bflo(k23), bfhi(c23) + bfhi(k23));               \
        u16* q = yb + ((i) * 272 + slp) * 4 + w;                                              \
        q[0] = (u16)p01; q[4] = (u16)(p01 >> 16); q[8] = (u16)p23; q[12] = (u16)(p23 >> 16);  \
    }
    EPI(a0, n0, 0) EPI(a1, n1, 1) EPI(a2, n2, 2) EPI(a3, n3, 3)
    __syncthreads();
    {
        const u32x2* yr = reinterpret_cast<const u32x2*>(smem + CV_Y) + t + (t >> 4);
        u32x2* dst = reinterpret_cast<u32x2*>(Y4) + (size_t)cq * S_;
        dst[256 * n0 + t] = yr[0];
        dst[256 * n1 + t] = yr[272];
        dst[256 * n2 + t] = yr[544];
        dst[256 * n3 + t] = yr[816];
    }
    STAMP(1, 4);
}

__device__ __forceinline__ float rowsum8(float v) {
    v += __builtin_bit_cast(float, __builtin_amdgcn_update_dpp(0, __builtin_bit_cast(int, v), 0x128, 0xf, 0xf, false));
    {
        const unsigned u = __builtin_bit_cast(unsigned, v);
        auto r = __builtin_amdgcn_permlane16_swap(u, u, false, false);
        v = __builtin_bit_cast(float, (unsigned)r[0]) + __builtin_bit_cast(float, (unsigned)r[1]);
    }
    {
        const unsigned u = __builtin_bit_cast(unsigned, v);
        auto r = __builtin_amdgcn_permlane32_swap(u, u, false, false);
        v = __builtin_bit_cast(float, (unsigned)r[0]) + __builtin_bit_cast(float, (unsigned)r[1]);
    }
    return v;
}
__device__ __forceinline__ f32x2 unpk(unsigned u) { f32x2 r = {bflo(u), bfhi(u)}; return r; }

__device__ __forceinline__ void g1_epilogue(const f32x4 (&ag)[4], const f32x4 (&au)[4], int rb, int ft, int l,
                                            u32x2* __restrict__ hpk, const f32x2* st2, u32x4 cgu, u32x4 cuu) {
    const int ks2 = ft >> 1;
    const int slot = (2 * (ft & 1) + (l >> 5)) * 16 + (l & 15);
    const f32x4 cg = __builtin_bit_cast(f32x4, cgu), cu = __builtin_bit_cast(f32x4, cuu);
    float sa[4], sb[4];
#pragma unroll
    for (int rt = 0; rt < 4; ++rt) {
        const f32x4 pp = *reinterpret_cast<const f32x4*>(st2 + 2 * (16 * rt + (l & 15)));
        const float mean2 = (pp[0] + pp[2]) * (1.0f / 512.0f);
        const float inv2 = __builtin_amdgcn_rsqf(fmaxf((pp[1] + pp[3]) * (1.0f / 512.0f) - mean2 * mean2, 0.f) + 1e-5f);
        sa[rt] = inv2;
        sb[rt] = -mean2 * inv2;
    }
    u32x2* dst = hpk + ((size_t)(rb * 4 * KS2 + ks2) * 64 + slot) * 2 + ((l >> 4) & 1);
#pragma unroll
    for (int rt = 0; rt < 4; ++rt) {
        const f32x4 g4 = ag[rt] * sa[rt] + cg * sb[rt];
        const f32x4 u4 = au[rt] * sa[rt] + cu * sb[rt];
        const unsigned g01 = pk2(g4[0], g4[1]), g23 = pk2(g4[2], g4[3]);
        const unsigned u01 = pk2(u4[0], u4[1]), u23 = pk2(u4[2], u4[3]);
        const float g0 = bflo(g01), g1 = bfhi(g01), g2 = bflo(g23), g3 = bfhi(g23);
        const float e0 = __expf(-g0), e1 = __expf(-g1), e2 = __expf(-g2), e3 = __expf(-g3);
        const float r0 = __builtin_amdgcn_rcpf(1.0f + e0), r1 = __builtin_amdgcn_rcpf(1.0f + e1);
        const float r2 = __builtin_amdgcn_rcpf(1.0f + e2), r3 = __builtin_amdgcn_rcpf(1.0f + e3);
        const unsigned s01 = pk2(g0 * r0, g1 * r1), s23 = pk2(g2 * r2, g3 * r3);
        u32x2 o;
        o[0] = pk2(bflo(s01) * bflo(u01), bfhi(s01) * bfhi(u01));
        o[1] = pk2(bflo(s23) * bflo(u23), bfhi(s23) * bfhi(u23));
        dst[(size_t)rt * KS2 * 128] = o;
    }
}

constexpr int G1_WIN = 7;
constexpr int G1_NL = 0;
__device__ constexpr bool g1_is_reg(int t) { return t < G1_WIN || t >= G1_WIN + G1_NL; }
__device__ constexpr int g1_ridx(int t) { return t < G1_WIN ? t : t - G1_NL; }
__device__ constexpr int g1_rstep(int r) { return r < G1_WIN ? r : r + G1_NL; }
__device__ constexpr int g1_younger(int t, int T) {
    int pos[40] = {};
    int n = 4;
    for (int u = 0; u < G1_WIN + G1_NL; ++u) { n += 2; pos[u] = n; }
    for (int u = 0; u < T; ++u) {
        if (u == t) return n - pos[t];
        if (g1_is_reg(u)) {
            const int nx = g1_rstep(g1_ridx(u) + G1_WIN);
            if (nx < T) { n += 2; pos[nx] = n; }
        }
        if (u == 15 && T > 16) n += 4;
    }
    return 0;
}
#define G1_WPTR(t) ((t) < 16 ? wbase : wbase2)
#define G1_LOADW(t)                                                                                                   \
    {                                                                                                                 \
        wf[g1_ridx(t) % G1_WIN][0] = gld16<((2 * ((t) & 15)) & 3) * 1024>(G1_WPTR(t), wo + ((2 * ((t) & 15)) >> 2) * 4096);     \
        wf[g1_ridx(t) % G1_WIN][1] = gld16<((2 * ((t) & 15) + 1) & 3) * 1024>(G1_WPTR(t), wo + ((2 * ((t) & 15)) >> 2) * 4096); \
    }
#define G1_STEP(t, T)                                                                         \
    {                                                                                         \
        u32x4 w0, w1;                                                                         \
        if (g1_is_reg(t)) {                                                                   \
            vm_wait2<g1_younger(t, T)>(wf[g1_ridx(t) % G1_WIN][0], wf[g1_ridx(t) % G1_WIN][1]); \
            w0 = wf[g1_ridx(t) % G1_WIN][0];                                                  \
            w1 = wf[g1_ridx(t) % G1_WIN][1];                                                  \
        } else {                                                                              \
            asm volatile("s_waitcnt vmcnt(%0)" :: "i"(g1_younger(t, T)) : "memory");          \
            w0 = wl[(2 * ((t) - G1_WIN)) * 64 + l];                                           \
            w1 = wl[(2 * ((t) - G1_WIN) + 1) * 64 + l];                                       \
        }                                                                                     \
        _Pragma("unroll") for (int rt = 0; rt < 4; ++rt) {                                    \
            const u32x4 bb = At[(rt * KS1 + ((t) & 15)) * 64 + l];                            \
            ag[rt] = mfma16(w0, bb, ag[rt]);                                                  \
            au[rt] = mfma16(w1, bb, au[rt]);                                                  \
        }                                                                                     \
        if (g1_is_reg(t) && g1_rstep(g1_ridx(t) + G1_WIN) < (T)) G1_LOADW(g1_rstep(g1_ridx(t) + G1_WIN)) \
    }
#define G1_STEPS16(b, T)                                                                                          \
    G1_STEP(b + 0, T) G1_STEP(b + 1, T) G1_STEP(b + 2, T) G1_STEP(b + 3, T) G1_STEP(b + 4, T) G1_STEP(b + 5, T)   \
    G1_STEP(b + 6, T) G1_STEP(b + 7, T) G1_STEP(b + 8, T) G1_STEP(b + 9, T) G1_STEP(b + 10, T) G1_STEP(b + 11, T) \
    G1_STEP(b + 12, T) G1_STEP(b + 13, T) G1_STEP(b + 14, T) G1_STEP(b + 15, T)

__device__ __forceinline__ void dma16(const void* gsrc, unsigned lds_dst) {
    unsigned keep;
    asm volatile("s_mov_b32 %0, m0\n\ts_mov_b32 m0, %2\n\ts_nop 0\n\tglobal_load_lds_dwordx4 %1, off\n\ts_mov_b32 m0, %0"
                 : "=&s"(keep) : "v"(gsrc), "s"(lds_dst) : "memory");
}

template <bool TWO>
__device__ __forceinline__ void g1_mfma_role(const u32x4* At, const f32x2* st2, u32x4* wl, const u32x4* wbase,
                                             const float* csum, int l, int rb, int ft, u32x2* __restrict__ hpk) {
    constexpr int T = TWO ? 32 : 16;
    const u32x4* wbase2 = wbase + (size_t)8 * (KS1 * 2 * 64);
    const unsigned wo = 16 * l;
    u32x4 wf[G1_WIN][2];
    const unsigned co = (16 * ft + 4 * (l >> 4)) * 4;
    u32x4 cs[4];
    cs[0] = gld16<0>(csum, co);
    cs[1] = gld16<0>(csum, co + F_ * 4);
    cs[2] = gld16<0>(csum, co + (TWO ? 512 : 0));
    cs[3] = gld16<0>(csum, co + F_ * 4 + (TWO ? 512 : 0));
    G1_LOADW(0) G1_LOADW(1) G1_LOADW(2) G1_LOADW(3) G1_LOADW(4)
    if (G1_WIN > 5) G1_LOADW(5)
    if (G1_WIN > 6) G1_LOADW(6)
    if (G1_NL > 0) {
        const unsigned lbase = __builtin_amdgcn_readfirstlane((unsigned)(size_t)wl);
#pragma unroll
        for (int i = 0; i < 2 * G1_NL; ++i) dma16(wbase + (2 * G1_WIN + i) * 64 + l, lbase + i * 1024);
    }
    STAMP(2, 4);
    __syncthreads();
    STAMP(2, 5);
    f32x4 ag[4], au[4];
#pragma unroll
    for (int r = 0; r < 4; ++r) { ag[r] = (f32x4){0.f, 0.f, 0.f, 0.f}; au[r] = ag[r]; }
    G1_STEPS16(0, T)
    vm_wait2<2 * (G1_WIN - 1) + 2>(cs[0], cs[1]);
    vm_wait2<2 * (G1_WIN - 1) + 2>(cs[2], cs[3]);
    STAMP(2, 6);
    g1_epilogue(ag, au, rb, ft, l, hpk, st2, cs[0], cs[1]);
    if (TWO) {
#pragma unroll
        for (int r = 0; r < 4; ++r) { ag[r] = (f32x4){0.f, 0.f, 0.f, 0.f}; au[r] = ag[r]; }
        G1_STEPS16(16, T)
        STAMP(2, 2);
        g1_epilogue(ag, au, rb, ft + 8, l, hpk, st2, cs[2], cs[3]);
    }
}

__global__ __launch_bounds__(1024, 4) void g1_kernel(const u32x2* __restrict__ x, const f32x2* __restrict__ part,
                                                     const u32x2* __restrict__ Y4, const u32x4* __restrict__ Wgu,
                                                     const float* __restrict__ csum, u32x2* __restrict__ x1out,
                                                     u32x2* __restrict__ hpk) {
    __shared__ u32x4 At[4 * KS1 * 64 + 64 + 8 * 2 * G1_NL * 64];
    f32x2* st2 = reinterpret_cast<f32x2*>(At + 4 * KS1 * 64);
    const int b = blockIdx.x;
    const int rb = (b & 7) * 4 + (b >> 6), cb = (b >> 3) & 7;
    const int l = threadIdx.x & 63;
    const int w = __builtin_amdgcn_readfirstlane(threadIdx.x >> 6);
    const int ntl = cb < 6 ? 11 : 10;
    const int ft0 = cb < 6 ? 11 * cb : 66 + 10 * (cb - 6);
    STAMP(2, 0);
    if (w < 8) {
        const int ip = l & 7, g = l >> 3;
        const int rt = w >> 1, dh = w & 1;
        const int row = rb * 64 + 16 * rt + 2 * ip;
        u32x4 xv[8], yv[8];
        u32x2 pa = gld8<0>(part, (unsigned)(row * 8 + g) * 8);
        u32x2 pb = gld8<64>(part, (unsigned)(row * 8 + g) * 8);
        const unsigned o0 = (unsigned)((64 * dh + g) * S_ + row) * 8;
#pragma unroll
        for (int c = 0; c < 8; ++c) xv[c] = gld16<0>(x, o0 + c * (8 * S_ * 8));
#pragma unroll
        for (int c = 0; c < 8; ++c) yv[c] = gld16<0>(Y4, o0 + c * (8 * S_ * 8));
        STAMP(2, 5);
        vm_wait2<16>(pa, pb);
        const float meanA = rowsum8(__builtin_bit_cast(float, (unsigned)pa[0])) * (1.0f / 512.0f);
        const float msqA = rowsum8(__builtin_bit_cast(float, (unsigned)pa[1])) * (1.0f / 512.0f);
        const float invA = 1.0f / sqrtf(fmaxf(msqA - meanA * meanA, 0.f) + 1e-5f);
        const float meanB = rowsum8(__builtin_bit_cast(float, (unsigned)pb[0])) * (1.0f / 512.0f);
        const float msqB = rowsum8(__builtin_bit_cast(float, (unsigned)pb[1])) * (1.0f / 512.0f);
        const float invB = 1.0f / sqrtf(fmaxf(msqB - meanB * meanB, 0.f) + 1e-5f);
        vm_wait8<8>(xv);
        STAMP(2, 1);
        vm_wait8<0>(yv);
        STAMP(2, 3);
        const f32x2 iA = {invA, invA}, bA = {-meanA * invA, -meanA * invA};
        const f32x2 iB = {invB, invB}, bB = {-meanB * invB, -meanB * invB};
        f32x2 sa = {0.f, 0.f}, sb = sa, qa = sa, qb = sa;
        u32x2* At2 = reinterpret_cast<u32x2*>(At);
#pragma unroll
        for (int c = 0; c < 8; ++c) {
            u32x4 o;
#pragma unroll
            for (int j = 0; j < 4; ++j) {
                const f32x2 xx = unpk(xv[c][j]);
                const f32x2 yy = unpk(yv[c][j]);
                const f32x2 q = (j < 2) ? (xx * iA + bA) : (xx * iB + bB);
                const f32x2 r = xx + q * yy;
                o[j] = pk2(r[0], r[1]);
                if (j < 2) { sa += r; qa += r * r; } else { sb += r; qb += r * r; }
            }
            const int unit = (rt * KS1 + 8 * dh + c) * 64 + (g >> 1) * 16 + 2 * ip;
            u32x2 oa = {o[0], o[1]}, ob = {o[2], o[3]};
            At2[2 * unit + (g & 1)] = oa;
            At2[2 * (unit + 1) + (g & 1)] = ob;
            if ((8 * dh + c) >> 1 == cb)
                *reinterpret_cast<u32x4*>(x1out + (size_t)(64 * dh + 8 * c + g) * S_ + row) = o;
        }
        const float s_a = rowsum8(sa[0] + sa[1]), q_a = rowsum8(qa[0] + qa[1]);
        const float s_b = rowsum8(sb[0] + sb[1]), q_b = rowsum8(qb[0] + qb[1]);
        if (g == 0) {
            f32x2 ea = {s_a, q_a}, eb = {s_b, q_b};
            st2[2 * (16 * rt + 2 * ip) + dh] = ea;
            st2[2 * (16 * rt + 2 * ip + 1) + dh] = eb;
        }
        STAMP(2, 4);
        if (w + 8 < ntl) g1_mfma_role<false>(At, st2, At, Wgu + (size_t)(ft0 + 8 + w) * (KS1 * 2 * 64), csum, l, rb, ft0 + 8 + w, hpk);
        else __syncthreads();
    } else {
        g1_mfma_role<false>(At, st2, At, Wgu + (size_t)(ft0 + w - 8) * (KS1 * 2 * 64), csum, l, rb, ft0 + w - 8, hpk);
    }
    STAMP(2, 7);
}

template <bool SIX>
__device__ __forceinline__ void g2_main(const u32x4* hbase, const u32x4* wbase, int l, f32x4 (&acc)[4][4]) {
    u32x4 af[5][4], bf[5][4];
    unsigned vo[4];
#pragma unroll
    for (int i = 0; i < 4; ++i) vo[i] = 16 * l + i * (KS2 * 1024);
#define G2_LOAD(st)                                                                                   \
    _Pragma("unroll") for (int i = 0; i < 4; ++i) {                                                   \
        af[(st) % 5][i] = gld16<((st) & 3) * 1024>(wbase, vo[i] + ((st) >> 2) * 4096);                \
        bf[(st) % 5][i] = gld16<((st) & 3) * 1024>(hbase, vo[i] + ((st) >> 2) * 4096);                \
    }
#define G2_STEP(st, nwait)                                                                            \
    {                                                                                                 \
        vm_wait8<nwait>(af[(st) % 5], bf[(st) % 5]);                                                  \
        _Pragma("unroll") for (int i = 0; i < 4; ++i)                                                 \
            _Pragma("unroll") for (int j = 0; j < 4; ++j)                                             \
                acc[i][j] = mfma16(af[(st) % 5][i], bf[(st) % 5][j], acc[i][j]);                      \
    }
    G2_LOAD(0) G2_LOAD(1) G2_LOAD(2) G2_LOAD(3) G2_LOAD(4)
    STAMP(3, 1);
    G2_STEP(0, 32)
    STAMP(3, 2);
    if (SIX) {
        G2_LOAD(5)
        G2_STEP(1, 32) G2_STEP(2, 24) G2_STEP(3, 16) G2_STEP(4, 8) G2_STEP(5, 0)
    } else {
        G2_STEP(1, 24) G2_STEP(2, 16) G2_STEP(3, 8) G2_STEP(4, 0)
    }
}

__device__ __forceinline__ float quadsum(float v) {
    {
        const unsigned u = __builtin_bit_cast(unsigned, v);
        auto r = __builtin_amdgcn_permlane16_swap(u, u, false, false);
        v = __builtin_bit_cast(float, (unsigned)r[0]) + __builtin_bit_cast(float, (unsigned)r[1]);
    }
    {
        const unsigned u = __builtin_bit_cast(unsigned, v);
        auto r = __builtin_amdgcn_permlane32_swap(u, u, false, false);
        v = __builtin_bit_cast(float, (unsigned)r[0]) + __builtin_bit_cast(float, (unsigned)r[1]);
    }
    return v;
}

template <bool STATS>
__global__ __launch_bounds__(512, 2) void g2_kernel(const u32x4* __restrict__ hpk, const u32x4* __restrict__ Wd,
                                                    const u32x2* __restrict__ xin, u32x2* __restrict__ xout,
                                                    f32x2* __restrict__ part) {
    __shared__ f32x4 red[8 * 16 * 64 + 128];
    f32x2* sred = reinterpret_cast<f32x2*>(red + 8 * 16 * 64);
    const int b = blockIdx.x;
    const int rb = (b & 7) * 4 + (b >> 6), cb = (b >> 3) & 7;
    const int l = threadIdx.x & 63;
    const int w = __builtin_amdgcn_readfirstlane(threadIdx.x >> 6);
    const int start = 5 * w + (w < 3 ? w : 3);
    const u32x4* hbase = hpk + ((size_t)(rb * 4) * KS2 + start) * 64;
    const u32x4* wbase = Wd + ((size_t)(cb * 4) * KS2 + start) * 64;
    const int n = cb * 64 + 16 * (w >> 1) + 4 * (l >> 4);
    const int s0 = rb * 64 + 32 * (w & 1) + (l & 15);
    const unsigned xo = (unsigned)((n >> 2) * S_ + s0) * 8;
    STAMP(3, 0);
    u32x2 xr0 = gld8<0>(xin, xo), xr1 = gld8<128>(xin, xo);
    f32x4 acc[4][4];
#pragma unroll
    for (int i = 0; i < 4; ++i)
#pragma unroll
        for (int j = 0; j < 4; ++j) acc[i][j] = (f32x4){0.f, 0.f, 0.f, 0.f};
    if (w < 3) g2_main<true>(hbase, wbase, l, acc);
    else g2_main<false>(hbase, wbase, l, acc);
    STAMP(3, 3);
#pragma unroll
    for (int i = 0; i < 4; ++i)
#pragma unroll
        for (int j = 0; j < 4; ++j) red[(w * 16 + i * 4 + j) * 64 + l] = acc[i][j];
    STAMP(3, 4);
    __syncthreads();
    STAMP(3, 5);
    vm_wait2<0>(xr0, xr1);
#pragma unroll
    for (int q = 0; q < 2; ++q) {
        const int tile = 2 * w + q;
        const u32x2 xr = q ? xr1 : xr0;
        f32x4 sum = red[tile * 64 + l];
#pragma unroll
        for (int p = 1; p < 8; ++p) sum += red[(p * 16 + tile) * 64 + l];
        u32x2 o;
        o[0] = pk2(bflo(xr[0]) + rbf(sum[0]), bfhi(xr[0]) + rbf(sum[1]));
        o[1] = pk2(bflo(xr[1]) + rbf(sum[2]), bfhi(xr[1]) + rbf(sum[3]));
        if (STATS) xout[(size_t)(n >> 2) * S_ + s0 + 16 * q] = o;
        else xout[((size_t)(s0 + 16 * q) * D_ + n) >> 2] = o;
        if (STATS) {
            const float v0 = bflo(o[0]), v1 = bfhi(o[0]), v2 = bflo(o[1]), v3 = bfhi(o[1]);
            const float sm = quadsum((v0 + v1) + (v2 + v3));
            const float sq = quadsum((v0 * v0 + v1 * v1) + (v2 * v2 + v3 * v3));
            if (l < 16) { f32x2 e = {sm, sq}; sred[(w >> 1) * 64 + 32 * (w & 1) + 16 * q + l] = e; }
        }
    }
    if (STATS) {
        __syncthreads();
        if (threadIdx.x < 64) {
            const f32x2 e = (sred[threadIdx.x] + sred[64 + threadIdx.x]) + (sred[128 + threadIdx.x] + sred[192 + threadIdx.x]);
            part[(size_t)(rb * 64 + threadIdx.x) * 8 + cb] = e;
        }
    }
    STAMP(3, 6);
}

extern "C" void kernel_launch(void* const* d_in, const int* in_sizes, int n_in,
                              void* d_out, int out_size, void* d_ws, size_t ws_size,
                              hipStream_t stream) {
    const u16* q       = (const u16*)d_in[0];
    const u16* k       = (const u16*)d_in[1];
    const u16* v       = (const u16*)d_in[2];
    const u16* shift_k = (const u16*)d_in[3];
    const u16* ssm_k   = (const u16*)d_in[4];
    const u16* ssm_D   = (const u16*)d_in[5];
    const u16* w_gate  = (const u16*)d_in[7];
    const u16* w_up    = (const u16*)d_in[8];
    const u16* w_down  = (const u16*)d_in[9];
    u16* out = (u16*)d_out;

    char* ws = (char*)d_ws;
    const size_t MB2 = (size_t)S_ * D_ * 2;
    u16*   kvR = (u16*)(ws);
    u16*   Y4  = (u16*)(ws + MB2);
    u16*   x1  = (u16*)(ws + 2 * MB2);
    u16*   x2  = (u16*)(ws + 3 * MB2);
    u16*   x3  = (u16*)(ws + 4 * MB2);
    u32x4* Wgu = (u32x4*)(ws + 5 * MB2);
    u32x4* Wd  = (u32x4*)(ws + 5 * MB2 + (size_t)NFT * KS1 * 2 * 1024);
    u32x4* hpk = (u32x4*)(ws + 5 * MB2 + (size_t)NFT * KS1 * 2 * 1024 + (size_t)32 * KS2 * 1024);

    char* tail = (char*)hpk + (size_t)128 * KS2 * 1024;
    float* csum  = (float*)tail;
    f32x2* part0 = (f32x2*)(tail + 16384);
    f32x2* part2 = (f32x2*)(tail + 16384 + 131072);

    u32x2* x0t = (u32x2*)(tail + 16384 + 2 * 131072);

    prep_kernel<<<256 + PACK_BLOCKS + 86 + 128, 256, 0, stream>>>(k, v, shift_k, w_gate, w_up, w_down, q, kvR, Wgu, Wd,
                                                                  csum, part0, x0t);
    conv_kernel<<<256, 256, 0, stream>>>(kvR, ssm_k, ssm_D, Y4);
    g1_kernel<<<256, 1024, 0, stream>>>(x0t, part0, (const u32x2*)Y4, Wgu, csum, (u32x2*)x1, (u32x2*)hpk);
    g2_kernel<true><<<256, 512, 0, stream>>>(hpk, Wd, (const u32x2*)x1, (u32x2*)x2, part2);
    g1_kernel<<<256, 1024, 0, stream>>>((const u32x2*)x2, part2, (const u32x2*)Y4, Wgu, csum, (u32x2*)x3, (u32x2*)hpk);
    g2_kernel<false><<<256, 512, 0, stream>>>(hpk, Wd, (const u32x2*)x3, (u32x2*)out, part2);
}
```

```cpp
#include <hip/hip_runtime.h>
#include <hip/hip_bf16.h>
#include <stdint.h>

typedef __attribute__((ext_vector_type(8))) __bf16   bf16x8;
typedef __attribute__((ext_vector_type(2))) __bf16   bf16x2;
typedef __attribute__((ext_vector_type(4))) float    f32x4;
typedef __attribute__((ext_vector_type(2))) float    f32x2;
typedef __attribute__((ext_vector_type(4))) unsigned u32x4;
typedef __attribute__((ext_vector_type(2))) unsigned u32x2;
typedef unsigned short u16;

#define STAMP(k, i)

constexpr int S_ = 2048, D_ = 512, F_ = 1376;
constexpr int NFT = 86;
constexpr int KS1 = 16;
constexpr int KS2 = 43;

__device__ __forceinline__ float bflo(unsigned u) { return __builtin_bit_cast(float, u << 16); }
__device__ __forceinline__ float bfhi(unsigned u) { return __builtin_bit_cast(float, u & 0xffff0000u); }
__device__ __forceinline__ unsigned pk2(float lo, float hi) {
    f32x2 v = {lo, hi};
    bf16x2 r = __builtin_convertvector(v, bf16x2);
    return __builtin_bit_cast(unsigned, r);
}
__device__ __forceinline__ float rbf(float x) { return bflo(pk2(x, x)); }
__device__ __forceinline__ f32x4 mfma16(u32x4 a, u32x4 b, f32x4 c) {
    return __builtin_amdgcn_mfma_f32_16x16x32_bf16(__builtin_bit_cast(bf16x8, a), __builtin_bit_cast(bf16x8, b), c, 0, 0, 0);
}

template <int IMM> __device__ __forceinline__ u32x4 gld16(const void* sbase, unsigned voff) {
    u32x4 r;
    asm volatile("global_load_dwordx4 %0, %1, %2 offset:%3" : "=v"(r) : "v"(voff), "s"(sbase), "i"(IMM) : "memory");
    return r;
}
template <int IMM> __device__ __forceinline__ u32x2 gld8(const void* sbase, unsigned voff) {
    u32x2 r;
    asm volatile("global_load_dwordx2 %0, %1, %2 offset:%3" : "=v"(r) : "v"(voff), "s"(sbase), "i"(IMM) : "memory");
    return r;
}
template <int N> __device__ __forceinline__ void vm_wait2(u32x4& a, u32x4& b) {
    asm volatile("s_waitcnt vmcnt(%2)" : "+v"(a), "+v"(b) : "i"(N) : "memory");
}
template <int N> __device__ __forceinline__ void vm_wait8(u32x4 (&a)[4], u32x4 (&b)[4]) {
    asm volatile("s_waitcnt vmcnt(%8)"
                 : "+v"(a[0]), "+v"(a[1]), "+v"(a[2]), "+v"(a[3]), "+v"(b[0]), "+v"(b[1]), "+v"(b[2]), "+v"(b[3])
                 : "i"(N) : "memory");
}
template <int N> __device__ __forceinline__ void vm_wait8(u32x4 (&a)[8]) {
    asm volatile("s_waitcnt vmcnt(%8)"
                 : "+v"(a[0]), "+v"(a[1]), "+v"(a[2]), "+v"(a[3]), "+v"(a[4]), "+v"(a[5]), "+v"(a[6]), "+v"(a[7])
                 : "i"(N) : "memory");
}
template <int N> __device__ __forceinline__ void vm_wait16(u32x2 (&a)[16]) {
    asm volatile("s_waitcnt vmcnt(%16)"
                 : "+v"(a[0]), "+v"(a[1]), "+v"(a[2]), "+v"(a[3]), "+v"(a[4]), "+v"(a[5]), "+v"(a[6]), "+v"(a[7]),
                   "+v"(a[8]), "+v"(a[9]), "+v"(a[10]), "+v"(a[11]), "+v"(a[12]), "+v"(a[13]), "+v"(a[14]), "+v"(a[15])
                 : "i"(N) : "memory");
}
template <int N> __device__ __forceinline__ void vm_wait1(u32x2& a) {
    asm volatile("s_waitcnt vmcnt(%1)" : "+v"(a) : "i"(N) : "memory");
}
template <int N> __device__ __forceinline__ void vm_wait2(u32x2& a, u32x2& b) {
    asm volatile("s_waitcnt vmcnt(%2)" : "+v"(a), "+v"(b) : "i"(N) : "memory");
}

constexpr int PACK_GU_THREADS = 2 * 64 * 688;
constexpr int PACK_D_THREADS  = 172 * 256;
constexpr int PACK_BLOCKS = (PACK_GU_THREADS + PACK_D_THREADS) / 256;

__device__ __forceinline__ void pack_item(const u16* __restrict__ src, int N, int ko, int np, u32x4* __restrict__ dst0) {
    unsigned w[8];
#pragma unroll
    for (int j = 0; j < 8; ++j) w[j] = *reinterpret_cast<const unsigned*>(src + (size_t)(8 * ko + j) * N + 2 * np);
    u32x4 o0, o1;
#pragma unroll
    for (int c = 0; c < 4; ++c) {
        o0[c] = (w[2 * c] & 0xffffu) | (w[2 * c + 1] << 16);
        o1[c] = (w[2 * c] >> 16) | (w[2 * c + 1] & 0xffff0000u);
    }
    dst0[0] = o0;
    dst0[1] = o1;
}

__global__ __launch_bounds__(256) void prep_kernel(const u16* __restrict__ k, const u16* __restrict__ v,
                                                   const u16* __restrict__ shift_k,
                                                   const u16* __restrict__ wg, const u16* __restrict__ wu,
                                                   const u16* __restrict__ wd,
                                                   const u16* __restrict__ x0,
                                                   u16* __restrict__ kvR, u32x4* __restrict__ Wgu,
                                                   u32x4* __restrict__ Wd, float* __restrict__ csum,
                                                   f32x2* __restrict__ part0, u32x2* __restrict__ x0t) {
    __shared__ unsigned tile[64 * 33];
    const int t = threadIdx.x;
    STAMP(0, 0);
    if (blockIdx.x < 256) {
        const int st = blockIdx.x >> 3, dt = blockIdx.x & 7;
        const int s0 = st * 64, d0 = dt * 64;
        const int c8 = t & 7, r = t >> 3;
        const int sA = s0 + 2 * r;
        const int dd = d0 + 8 * c8;
        u32x4 kr[5], vr[2], wv[4];
#pragma unroll
        for (int i = 0; i < 5; ++i) {
            const int s = sA - 3 + i;
            u32x4 z = {0u, 0u, 0u, 0u};
            kr[i] = (s >= 0) ? *reinterpret_cast<const u32x4*>(k + (size_t)s * D_ + dd) : z;
        }
#pragma unroll
        for (int i = 0; i < 2; ++i) vr[i] = *reinterpret_cast<const u32x4*>(v + (size_t)(sA + i) * D_ + dd);
#pragma unroll
        for (int i = 0; i < 4; ++i) wv[i] = *reinterpret_cast<const u32x4*>(shift_k + (size_t)dd * 4 + 8 * i);
#pragma unroll
        for (int j = 0; j < 8; ++j) {
            const unsigned wa = wv[j >> 1][(j & 1) * 2], wb = wv[j >> 1][(j & 1) * 2 + 1];
            const float w0 = bflo(wa), w1 = bfhi(wa), w2 = bflo(wb), w3 = bfhi(wb);
            float kk[5];
#pragma unroll
            for (int i = 0; i < 5; ++i) kk[i] = (j & 1) ? bfhi(kr[i][j >> 1]) : bflo(kr[i][j >> 1]);
            float res[2];
#pragma unroll
            for (int q = 0; q < 2; ++q) {
                float acc = rbf(kk[q] * w3);
                acc = rbf(acc + rbf(kk[q + 1] * w2));
                acc = rbf(acc + rbf(kk[q + 2] * w1));
                acc = rbf(acc + rbf(kk[q + 3] * w0));
                const float vv = (j & 1) ? bfhi(vr[q][j >> 1]) : bflo(vr[q][j >> 1]);
                res[q] = acc * vv;
            }
            tile[(8 * c8 + j) * 33 + (31 - r)] = pk2(res[1], res[0]);
        }
        STAMP(0, 1);
        __syncthreads();
#pragma unroll
        for (int i = 0; i < 2; ++i) {
            const int idx = t + 256 * i;
            const int d = idx >> 3, q = idx & 7;
            u32x4 o;
#pragma unroll
            for (int c = 0; c < 4; ++c) o[c] = tile[d * 33 + 4 * q + c];
            *reinterpret_cast<u32x4*>(kvR + (size_t)(d0 + d) * S_ + (S_ - 64 - s0) + 8 * q) = o;
        }
    } else if (blockIdx.x >= 256 + PACK_BLOCKS + 86) {
        const int r0 = (blockIdx.x - (256 + PACK_BLOCKS + 86)) * 16;
        const int row = t & 15;
        f32x2* sp = reinterpret_cast<f32x2*>(tile);
        u32x4 a[4];
#pragma unroll
        for (int i = 0; i < 4; ++i) a[i] = *reinterpret_cast<const u32x4*>(x0 + (size_t)(r0 + row) * D_ + 8 * ((t >> 4) + 16 * i));
#pragma unroll
        for (int i = 0; i < 4; ++i) {
            const int o = (t >> 4) + 16 * i;
            u32x2 lo = {a[i][0], a[i][1]}, hi = {a[i][2], a[i][3]};
            x0t[(size_t)(2 * o) * S_ + r0 + row] = lo;
            x0t[(size_t)(2 * o + 1) * S_ + r0 + row] = hi;
            float sm = 0.f, sq = 0.f;
#pragma unroll
            for (int j = 0; j < 4; ++j) {
                const float l0 = bflo(a[i][j]), h0 = bfhi(a[i][j]);
                sm += l0 + h0;
                sq += l0 * l0 + h0 * h0;
            }
            f32x2 e = {sm, sq};
            sp[row * 64 + o] = e;
        }
        __syncthreads();
        if (t < 128) {
            const int rr = t >> 3, g = t & 7;
            f32x2 e = sp[rr * 64 + 8 * g];
#pragma unroll
            for (int j = 1; j < 8; ++j) e += sp[rr * 64 + 8 * g + j];
            part0[(size_t)(r0 + rr) * 8 + g] = e;
        }
    } else if (blockIdx.x >= 256 + PACK_BLOCKS) {
        const int cbk = blockIdx.x - (256 + PACK_BLOCKS);
        const int m = cbk / 43, f0 = (cbk % 43) * 32;
        const u16* src = m ? wu : wg;
        const int np = t & 15, kg = t >> 4;
        unsigned wv[32];
#pragma unroll
        for (int j = 0; j < 32; ++j) wv[j] = *reinterpret_cast<const unsigned*>(src + (size_t)(32 * kg + j) * F_ + f0 + 2 * np);
        float s0 = 0.f, s1 = 0.f;
#pragma unroll
        for (int j = 0; j < 32; ++j) { s0 += bflo(wv[j]); s1 += bfhi(wv[j]); }
        float* red = reinterpret_cast<float*>(tile);
        red[kg * 33 + 2 * np] = s0;
        red[kg * 33 + 2 * np + 1] = s1;
        __syncthreads();
        if (t < 32) {
            float a = 0.f;
#pragma unroll
            for (int j = 0; j < 16; ++j) a += red[j * 33 + t];
            csum[m * F_ + f0 + t] = a;
        }
    } else {
        int id = (blockIdx.x - 256) * 256 + t;
        if (id < PACK_GU_THREADS) {
            const int np = id % 688;
            const int rest = id / 688;
            const int ko = rest & 63, m = rest >> 6;
            const int n = 2 * np;
            const int ft = n >> 4, ks = ko >> 2, g = ko & 3;
            pack_item(m ? wu : wg, F_, ko, np, Wgu + ((size_t)((ft * KS1 + ks) * 2 + m) * 64 + g * 16 + (n & 15)));
        } else {
            id -= PACK_GU_THREADS;
            const int np = id & 255;
            const int ko = id >> 8;
            const int n = 2 * np;
            const int nt = n >> 4, ks = ko >> 2, g = ko & 3;
            pack_item(wd, D_, ko, np, Wd + ((size_t)(nt * KS2 + ks) * 64 + g * 16 + (n & 15)));
        }
    }
    STAMP(0, 2);
}

constexpr int CV_C = 4160;
constexpr int CV_U = 4 * CV_C;
constexpr int CV_CH = CV_U + 4608;
constexpr int CV_Y = 4 * CV_CH;

template <int OFF> __device__ __forceinline__ u32x2 lds8(unsigned addr) {
    u32x2 r;
    asm volatile("ds_read_b64 %0, %1 offset:%2" : "=v"(r) : "v"(addr), "i"(OFF) : "memory");
    return r;
}
template <int OFF> __device__ __forceinline__ u32x4 lds16(unsigned addr) {
    u32x4 r;
    asm volatile("ds_read_b128 %0, %1 offset:%2" : "=v"(r) : "v"(addr), "i"(OFF) : "memory");
    return r;
}
__device__ __forceinline__ void lgkm_wait(u32x2 (&a)[16], u32x4 (&b)[8]) {
    asm volatile("s_waitcnt lgkmcnt(0)"
                 : "+v"(a[0]), "+v"(a[1]), "+v"(a[2]), "+v"(a[3]), "+v"(a[4]), "+v"(a[5]), "+v"(a[6]), "+v"(a[7]),
                   "+v"(a[8]), "+v"(a[9]), "+v"(a[10]), "+v"(a[11]), "+v"(a[12]), "+v"(a[13]), "+v"(a[14]), "+v"(a[15]),
                   "+v"(b[0]), "+v"(b[1]), "+v"(b[2]), "+v"(b[3]), "+v"(b[4]), "+v"(b[5]), "+v"(b[6]), "+v"(b[7])
                 :: "memory");
}
template <int BLK, int GBLK>
__device__ __forceinline__ void conv_load(unsigned fa, unsigned ga, u32x2 (&F)[16], u32x4 (&G)[8]) {
#define CVL(i) F[2 * (i)] = lds8<64 * (8 * BLK + (i))>(fa); F[2 * (i) + 1] = lds8<64 * (8 * BLK + (i)) + 8>(fa); \
               G[i] = lds16<64 * (8 * (GBLK + 7) + (i))>(ga);
    CVL(0) CVL(1) CVL(2) CVL(3) CVL(4) CVL(5) CVL(6) CVL(7)
#undef CVL
}
__device__ __forceinline__ u32x4 cat(u32x2 a, u32x2 b) { u32x4 r = {a[0], a[1], b[0], b[1]}; return r; }

template <int N, int T>
__device__ __forceinline__ void conv_step(unsigned fa, unsigned ga, u32x2 (&Fr)[3][16], u32x4 (&Gr)[2][8], f32x4& acc1,
                                          f32x4& acc0) {
    if (T >= 0) lgkm_wait(Fr[T % 3], Gr[T & 1]);
    if (T + 1 <= N + 1) conv_load<T + 1, T - N>(fa, ga, Fr[(T + 1) % 3], Gr[(T + 1) & 1]);
    __builtin_amdgcn_sched_barrier(0);
    if (T >= 0) {
#pragma unroll
        for (int i = 0; i < 8; ++i) {
            acc1 = mfma16(cat(Fr[T % 3][2 * i], Fr[T % 3][2 * i + 1]), Gr[T & 1][i], acc1);
            if (T >= 1) acc0 = mfma16(cat(Fr[(T + 2) % 3][2 * i], Fr[(T + 2) % 3][2 * i + 1]), Gr[T & 1][i], acc0);
        }
    }
    __builtin_amdgcn_sched_barrier(0);
    if constexpr (T < N + 1) conv_step<N, T + 1>(fa, ga, Fr, Gr, acc1, acc0);
}
template <int N>
__device__ __forceinline__ void conv_task(unsigned fa, unsigned ga, f32x4& acc1, f32x4& acc0) {
    u32x2 Fr[3][16];
    u32x4 Gr[2][8];
    conv_step<N, -1>(fa, ga, Fr, Gr, acc1, acc0);
}

__device__ __forceinline__ unsigned sh16(unsigned lo, unsigned hi) { return (lo >> 16) | (hi << 16); }

__global__ __launch_bounds__(256) void conv_kernel(const u16* __restrict__ kvR, const u16* __restrict__ ssm_k,
                                                   const u16* __restrict__ ssm_D, u16* __restrict__ Y4) {
    __shared__ __attribute__((aligned(16))) char smem[CV_Y + 8704];
    const int t = threadIdx.x, l = t & 63;
    const int w = __builtin_amdgcn_readfirstlane(t >> 6);
    const int cq = blockIdx.x >> 1, half = blockIdx.x & 1;
    const int ch = 4 * cq + w;
    char* base = smem + w * CV_CH;
    STAMP(1, 0);
    const float Dv = bflo((unsigned)ssm_D[ch]);
    {
        const u16* hp = ssm_k + (size_t)ch * S_;
        const u16* up = kvR + (size_t)ch * S_;
        u32x4 hv[4], uv[4];
        u32x2 hx[4];
#pragma unroll
        for (int i = 0; i < 4; ++i) {
            const int m = l + 64 * i;
            hv[i] = *reinterpret_cast<const u32x4*>(hp + 8 * m);
            uv[i] = *reinterpret_cast<const u32x4*>(up + 8 * m);
            const u32x2 zz = {0u, 0u};
            hx[i] = (m < 255) ? *reinterpret_cast<const u32x2*>(hp + 8 * m + 8) : zz;
        }
        const u32x4 z = {0u, 0u, 0u, 0u};
        if (l < 32) *reinterpret_cast<u32x4*>(base + CV_U + 4096 + 16 * l) = z;
        if (l < 4) *reinterpret_cast<u32x4*>(base + l * CV_C) = z;
        if (l == 0) {
            const unsigned h01 = hv[0][0], h23 = hv[0][1];
            u32x4 c0 = z, c1 = z, c2 = z, c3 = z;
            c0[3] = h01 << 16;
            c1[3] = h01;
            c2[2] = h01 << 16; c2[3] = sh16(h01, h23);
            c3[2] = h01;       c3[3] = h23;
            *reinterpret_cast<u32x4*>(base + 0 * CV_C + 16) = c0;
            *reinterpret_cast<u32x4*>(base + 1 * CV_C + 16) = c1;
            *reinterpret_cast<u32x4*>(base + 2 * CV_C + 16) = c2;
            *reinterpret_cast<u32x4*>(base + 3 * CV_C + 16) = c3;
        }
#pragma unroll
        for (int i = 0; i < 4; ++i) {
            const int m = l + 64 * i;
            const unsigned d0 = hv[i][0], d1 = hv[i][1], d2 = hv[i][2], d3 = hv[i][3], d4 = hx[i][0], d5 = hx[i][1];
            const unsigned e0 = sh16(d0, d1), e1 = sh16(d1, d2), e2 = sh16(d2, d3), e3 = sh16(d3, d4), e4 = sh16(d4, d5);
            u32x4 c0 = {e0, e1, e2, e3}, c1 = {d1, d2, d3, d4}, c2 = {e1, e2, e3, e4}, c3 = {d2, d3, d4, d5};
            *reinterpret_cast<u32x4*>(base + 0 * CV_C + 32 + 16 * m) = c0;
            *reinterpret_cast<u32x4*>(base + 1 * CV_C + 32 + 16 * m) = c1;
            *reinterpret_cast<u32x4*>(base + 2 * CV_C + 32 + 16 * m) = c2;
            *reinterpret_cast<u32x4*>(base + 3 * CV_C + 32 + 16 * m) = c3;
            *reinterpret_cast<u32x4*>(base + CV_U + 16 * m) = uv[i];
        }
    }
    STAMP(1, 1);
    __syncthreads();
    STAMP(1, 2);

    const int o0 = (l & 15) + 8 * (l >> 4);
    const unsigned fp = (unsigned)(size_t)(base + (l & 3) * CV_C + 2 * (o0 & ~3));
    const unsigned gp = (unsigned)(size_t)(base + CV_U + 2 * (2032 - 16 * (l & 15) + 8 * (l >> 4)) - 64 * 56);
    f32x4 a0 = {0.f, 0.f, 0.f, 0.f}, a1 = a0, a2 = a0, a3 = a0;
    int n0, n1, n2, n3;
    if (half == 0) {
        n0 = 7; n1 = 6; n2 = 1; n3 = 0;
        conv_task<6>(fp, gp, a0, a1);
        conv_task<0>(fp, gp, a2, a3);
    } else {
        n0 = 5; n1 = 4; n2 = 3; n3 = 2;
        conv_task<4>(fp, gp, a0, a1);
        conv_task<2>(fp, gp, a2, a3);
    }
    STAMP(1, 3);
    u16* yb = reinterpret_cast<u16*>(smem + CV_Y);
    const int sl = 16 * (l & 15) + 4 * (l >> 4);
    const int slp = sl + (l & 15);
#define EPI(acc, n, i)                                                                        \
    {                                                                                         \
        const int s = 256 * (n) + sl;                                                         \
        const u32x2 uu = *reinterpret_cast<const u32x2*>(base + CV_U + 2 * (2044 - s));       \
        const float u0 = bfhi(uu[1]), u1 = bflo(uu[1]), u2 = bfhi(uu[0]), u3 = bflo(uu[0]);   \
        const unsigned c01 = pk2(acc[0], acc[1]), c23 = pk2(acc[2], acc[3]);                  \
        const unsigned k01 = pk2(u0 * Dv, u1 * Dv), k23 = pk2(u2 * Dv, u3 * Dv);              \
        const unsigned p01 = pk2(bflo(c01) + bflo(k01), bfhi(c01) + bfhi(k01));               \
        const unsigned p23 = pk2(bflo(c23) + bflo(k23), bfhi(c23) + bfhi(k23));               \
        u16* q = yb + ((i) * 272 + slp) * 4 + w;                                              \
        q[0] = (u16)p01; q[4] = (u16)(p01 >> 16); q[8] = (u16)p23; q[12] = (u16)(p23 >> 16);  \
    }
    EPI(a0, n0, 0) EPI(a1, n1, 1) EPI(a2, n2, 2) EPI(a3, n3, 3)
    __syncthreads();
    {
        const u32x2* yr = reinterpret_cast<const u32x2*>(smem + CV_Y) + t + (t >> 4);
        u32x2* dst = reinterpret_cast<u32x2*>(Y4) + (size_t)cq * S_;
        dst[256 * n0 + t] = yr[0];
        dst[256 * n1 + t] = yr[272];
        dst[256 * n2 + t] = yr[544];
        dst[256 * n3 + t] = yr[816];
    }
    STAMP(1, 4);
}

__device__ __forceinline__ float rowsum8(float v) {
    v += __builtin_bit_cast(float, __builtin_amdgcn_update_dpp(0, __builtin_bit_cast(int, v), 0x128, 0xf, 0xf, false));
    {
        const unsigned u = __builtin_bit_cast(unsigned, v);
        auto r = __builtin_amdgcn_permlane16_swap(u, u, false, false);
        v = __builtin_bit_cast(float, (unsigned)r[0]) + __builtin_bit_cast(float, (unsigned)r[1]);
    }
    {
        const unsigned u = __builtin_bit_cast(unsigned, v);
        auto r = __builtin_amdgcn_permlane32_swap(u, u, false, false);
        v = __builtin_bit_cast(float, (unsigned)r[0]) + __builtin_bit_cast(float, (unsigned)r[1]);
    }
    return v;
}
__device__ __forceinline__ f32x2 unpk(unsigned u) { f32x2 r = {bflo(u), bfhi(u)}; return r; }

__device__ __forceinline__ void g1_epilogue(const f32x4 (&ag)[4], const f32x4 (&au)[4], int rb, int ft, int l,
                                            u32x2* __restrict__ hpk, const f32x2* st2, u32x4 cgu, u32x4 cuu) {
    const int ks2 = ft >> 1;
    const int slot = (2 * (ft & 1) + (l >> 5)) * 16 + (l & 15);
    const f32x4 cg = __builtin_bit_cast(f32x4, cgu), cu = __builtin_bit_cast(f32x4, cuu);
    float sa[4], sb[4];
#pragma unroll
    for (int rt = 0; rt < 4; ++rt) {
        const f32x4 pp = *reinterpret_cast<const f32x4*>(st2 + 2 * (16 * rt + (l & 15)));
        const float mean2 = (pp[0] + pp[2]) * (1.0f / 512.0f);
        const float inv2 = __builtin_amdgcn_rsqf(fmaxf((pp[1] + pp[3]) * (1.0f / 512.0f) - mean2 * mean2, 0.f) + 1e-5f);
        sa[rt] = inv2;
        sb[rt] = -mean2 * inv2;
    }
    u32x2* dst = hpk + ((size_t)(rb * 4 * KS2 + ks2) * 64 + slot) * 2 + ((l >> 4) & 1);
#pragma unroll
    for (int rt = 0; rt < 4; ++rt) {
        const f32x4 g4 = ag[rt] * sa[rt] + cg * sb[rt];
        const f32x4 u4 = au[rt] * sa[rt] + cu * sb[rt];
        const unsigned g01 = pk2(g4[0], g4[1]), g23 = pk2(g4[2], g4[3]);
        const unsigned u01 = pk2(u4[0], u4[1]), u23 = pk2(u4[2], u4[3]);
        const float g0 = bflo(g01), g1 = bfhi(g01), g2 = bflo(g23), g3 = bfhi(g23);
        const float e0 = __expf(-g0), e1 = __expf(-g1), e2 = __expf(-g2), e3 = __expf(-g3);
        const float r0 = __builtin_amdgcn_rcpf(1.0f + e0), r1 = __builtin_amdgcn_rcpf(1.0f + e1);
        const float r2 = __builtin_amdgcn_rcpf(1.0f + e2), r3 = __builtin_amdgcn_rcpf(1.0f + e3);
        const unsigned s01 = pk2(g0 * r0, g1 * r1), s23 = pk2(g2 * r2, g3 * r3);
        u32x2 o;
        o[0] = pk2(bflo(s01) * bflo(u01), bfhi(s01) * bfhi(u01));
        o[1] = pk2(bflo(s23) * bflo(u23), bfhi(s23) * bfhi(u23));
        dst[(size_t)rt * KS2 * 128] = o;
    }
}

constexpr int G1_WIN = 6;
constexpr int G1_NL = 0;
__device__ constexpr bool g1_is_reg(int t) { return t < G1_WIN || t >= G1_WIN + G1_NL; }
__device__ constexpr int g1_ridx(int t) { return t < G1_WIN ? t : t - G1_NL; }
__device__ constexpr int g1_rstep(int r) { return r < G1_WIN ? r : r + G1_NL; }
__device__ constexpr int g1_younger(int t, int T) {
    int pos[40] = {};
    int n = 2;
    for (int u = 0; u < G1_WIN + G1_NL; ++u) { n += 2; pos[u] = n; }
    for (int u = 0; u < T; ++u) {
        if (u == t) return n - pos[t];
        if (g1_is_reg(u)) {
            const int nx = g1_rstep(g1_ridx(u) + G1_WIN);
            if (nx < T) { n += 2; pos[nx] = n; }
        }
        if (u == 15 && T > 16) n += 4;
    }
    return 0;
}
__device__ constexpr int g1_ks(int t) { return ((t & 15) < 4) ? (t & 15) : ((t & 15) < 8) ? (t & 15) + 4 : ((t & 15) < 12) ? (t & 15) - 4 : (t & 15); }
#define G1_WPTR(t) ((t) < 16 ? wbase : wbase2)
#define G1_LOADW(t)                                                                                                   \
    {                                                                                                                 \
        wf[g1_ridx(t) % G1_WIN][0] = gld16<((2 * g1_ks(t)) & 3) * 1024>(G1_WPTR(t), wo + ((2 * g1_ks(t)) >> 2) * 4096);     \
        wf[g1_ridx(t) % G1_WIN][1] = gld16<((2 * g1_ks(t) + 1) & 3) * 1024>(G1_WPTR(t), wo + ((2 * g1_ks(t)) >> 2) * 4096); \
    }
#define G1_STEP(t, T)                                                                         \
    {                                                                                         \
        u32x4 w0, w1;                                                                         \
        if (g1_is_reg(t)) {                                                                   \
            vm_wait2<g1_younger(t, T)>(wf[g1_ridx(t) % G1_WIN][0], wf[g1_ridx(t) % G1_WIN][1]); \
            w0 = wf[g1_ridx(t) % G1_WIN][0];                                                  \
            w1 = wf[g1_ridx(t) % G1_WIN][1];                                                  \
        } else {                                                                              \
            asm volatile("s_waitcnt vmcnt(%0)" :: "i"(g1_younger(t, T)) : "memory");          \
            w0 = wl[(2 * ((t) - G1_WIN)) * 64 + l];                                           \
            w1 = wl[(2 * ((t) - G1_WIN) + 1) * 64 + l];                                       \
        }                                                                                     \
        _Pragma("unroll") for (int rt = 0; rt < 4; ++rt) {                                    \
            const u32x4 bb = At[(rt * KS1 + g1_ks(t)) * 64 + l];                            \
            ag[rt] = mfma16(w0, bb, ag[rt]);                                                  \
            au[rt] = mfma16(w1, bb, au[rt]);                                                  \
        }                                                                                     \
        if (g1_is_reg(t) && g1_rstep(g1_ridx(t) + G1_WIN) < (T)) G1_LOADW(g1_rstep(g1_ridx(t) + G1_WIN)) \
    }
#define G1_STEPS8(b, T)                                                                                           \
    G1_STEP(b + 0, T) G1_STEP(b + 1, T) G1_STEP(b + 2, T) G1_STEP(b + 3, T) G1_STEP(b + 4, T) G1_STEP(b + 5, T)   \
    G1_STEP(b + 6, T) G1_STEP(b + 7, T)

__device__ __forceinline__ void dma16(const void* gsrc, unsigned lds_dst) {
    unsigned keep;
    asm volatile("s_mov_b32 %0, m0\n\ts_mov_b32 m0, %2\n\ts_nop 0\n\tglobal_load_lds_dwordx4 %1, off\n\ts_mov_b32 m0, %0"
                 : "=&s"(keep) : "v"(gsrc), "s"(lds_dst) : "memory");
}

template <bool LATE>
__device__ __forceinline__ void g1_mfma_role(const u32x4* At, const f32x2* st2, u32x4* wl, const u32x4* wbase,
                                             const float* csum, int l, int rb, int ft, u32x2* __restrict__ hpk) {
    constexpr int T = 16;
    const u32x4* wbase2 = wbase;
    const unsigned wo = 16 * l;
    u32x4 wf[G1_WIN][2];
    const unsigned co = (16 * ft + 4 * (l >> 4)) * 4;
    u32x4 cs[2];
    cs[0] = gld16<0>(csum, co);
    cs[1] = gld16<0>(csum, co + F_ * 4);
    G1_LOADW(0) G1_LOADW(1) G1_LOADW(2) G1_LOADW(3)
    STAMP(2, 4);
    __syncthreads();
    STAMP(2, 5);
    G1_LOADW(4)
    if (G1_WIN > 5) G1_LOADW(5)
    if (G1_WIN > 6) G1_LOADW(6)
    if (G1_WIN > 7) G1_LOADW(7)
    if (G1_WIN > 8) G1_LOADW(8)
    f32x4 ag[4], au[4];
#pragma unroll
    for (int r = 0; r < 4; ++r) { ag[r] = (f32x4){0.f, 0.f, 0.f, 0.f}; au[r] = ag[r]; }
    G1_STEPS8(0, T)
    if (!LATE) __syncthreads();
    G1_STEPS8(8, T)
    vm_wait2<0>(cs[0], cs[1]);
    STAMP(2, 6);
    g1_epilogue(ag, au, rb, ft, l, hpk, st2, cs[0], cs[1]);
}

__global__ __launch_bounds__(1024, 4) void g1_kernel(const u32x2* __restrict__ x, const f32x2* __restrict__ part,
                                                     const u32x2* __restrict__ Y4, const u32x4* __restrict__ Wgu,
                                                     const float* __restrict__ csum, u32x2* __restrict__ x1out,
                                                     u32x2* __restrict__ hpk) {
    __shared__ u32x4 At[4 * KS1 * 64 + 64 + 8 * 2 * G1_NL * 64];
    f32x2* st2 = reinterpret_cast<f32x2*>(At + 4 * KS1 * 64);
    const int b = blockIdx.x;
    const int rb = (b & 7) * 4 + (b >> 6), cb = (b >> 3) & 7;
    const int l = threadIdx.x & 63;
    const int w = __builtin_amdgcn_readfirstlane(threadIdx.x >> 6);
    const int ntl = cb < 6 ? 11 : 10;
    const int ft0 = cb < 6 ? 11 * cb : 66 + 10 * (cb - 6);
    STAMP(2, 0);
    if (w < 8) {
        const int ip = l & 7, g = l >> 3;
        const int rt = w >> 1, dh = w & 1;
        const int row = rb * 64 + 16 * rt + 2 * ip;
        u32x4 xv[8], yv[8];
        u32x2 pa = gld8<0>(part, (unsigned)(row * 8 + g) * 8);
        u32x2 pb = gld8<64>(part, (unsigned)(row * 8 + g) * 8);
        const unsigned o0 = (unsigned)((64 * dh + g) * S_ + row) * 8;
#pragma unroll
        for (int c = 0; c < 8; ++c) {
            xv[c] = gld16<0>(x, o0 + c * (8 * S_ * 8));
            yv[c] = gld16<0>(Y4, o0 + c * (8 * S_ * 8));
        }
        STAMP(2, 5);
        vm_wait2<16>(pa, pb);
        const float meanA = rowsum8(__builtin_bit_cast(float, (unsigned)pa[0])) * (1.0f / 512.0f);
        const float msqA = rowsum8(__builtin_bit_cast(float, (unsigned)pa[1])) * (1.0f / 512.0f);
        const float invA = 1.0f / sqrtf(fmaxf(msqA - meanA * meanA, 0.f) + 1e-5f);
        const float meanB = rowsum8(__builtin_bit_cast(float, (unsigned)pb[0])) * (1.0f / 512.0f);
        const float msqB = rowsum8(__builtin_bit_cast(float, (unsigned)pb[1])) * (1.0f / 512.0f);
        const float invB = 1.0f / sqrtf(fmaxf(msqB - meanB * meanB, 0.f) + 1e-5f);
        const f32x2 iA = {invA, invA}, bA = {-meanA * invA, -meanA * invA};
        const f32x2 iB = {invB, invB}, bB = {-meanB * invB, -meanB * invB};
        f32x2 sa = {0.f, 0.f}, sb = sa, qa = sa, qb = sa;
        u32x2* At2 = reinterpret_cast<u32x2*>(At);
#define G1_TRANS(c)                                                                                   \
        {                                                                                             \
            vm_wait2<14 - 2 * (c)>(xv[c], yv[c]);                                                     \
            u32x4 o;                                                                                  \
            _Pragma("unroll") for (int j = 0; j < 4; ++j) {                                           \
                const f32x2 xx = unpk(xv[c][j]);                                                      \
                const f32x2 yy = unpk(yv[c][j]);                                                      \
                const f32x2 q = (j < 2) ? (xx * iA + bA) : (xx * iB + bB);                            \
                const f32x2 r = xx + q * yy;                                                          \
                o[j] = pk2(r[0], r[1]);                                                               \
                if (j < 2) { sa += r; qa += r * r; } else { sb += r; qb += r * r; }                   \
            }                                                                                         \
            const int unit = (rt * KS1 + 8 * dh + (c)) * 64 + (g >> 1) * 16 + 2 * ip;                 \
            u32x2 oa = {o[0], o[1]}, ob = {o[2], o[3]};                                               \
            At2[2 * unit + (g & 1)] = oa;                                                             \
            At2[2 * (unit + 1) + (g & 1)] = ob;                                                       \
            xv[c] = o;                                                                                \
        }
        G1_TRANS(0) G1_TRANS(1) G1_TRANS(2) G1_TRANS(3)
        STAMP(2, 1);
        __syncthreads();
        STAMP(2, 3);
        G1_TRANS(4) G1_TRANS(5) G1_TRANS(6) G1_TRANS(7)
#pragma unroll
        for (int c = 0; c < 8; ++c)
            if ((8 * dh + c) >> 1 == cb)
                *reinterpret_cast<u32x4*>(x1out + (size_t)(64 * dh + 8 * c + g) * S_ + row) = xv[c];
        const float s_a = rowsum8(sa[0] + sa[1]), q_a = rowsum8(qa[0] + qa[1]);
        const float s_b = rowsum8(sb[0] + sb[1]), q_b = rowsum8(qb[0] + qb[1]);
        if (g == 0) {
            f32x2 ea = {s_a, q_a}, eb = {s_b, q_b};
            st2[2 * (16 * rt + 2 * ip) + dh] = ea;
            st2[2 * (16 * rt + 2 * ip + 1) + dh] = eb;
        }
        STAMP(2, 4);
        if (w + 8 < ntl) g1_mfma_role<true>(At, st2, At, Wgu + (size_t)(ft0 + 8 + w) * (KS1 * 2 * 64), csum, l, rb, ft0 + 8 + w, hpk);
        else __syncthreads();
    } else {
        g1_mfma_role<false>(At, st2, At, Wgu + (size_t)(ft0 + w - 8) * (KS1 * 2 * 64), csum, l, rb, ft0 + w - 8, hpk);
    }
    STAMP(2, 7);
}

template <bool SIX>
__device__ __forceinline__ void g2_main(const u32x4* hbase, const u32x4* wbase, int l, f32x4 (&acc)[4][4]) {
    u32x4 af[5][4], bf[5][4];
    unsigned vo[4];
#pragma unroll
    for (int i = 0; i < 4; ++i) vo[i] = 16 * l + i * (KS2 * 1024);
#define G2_LOAD(st)                                                                                   \
    _Pragma("unroll") for (int i = 0; i < 4; ++i) {                                                   \
        af[(st) % 5][i] = gld16<((st) & 3) * 1024>(wbase, vo[i] + ((st) >> 2) * 4096);                \
        bf[(st) % 5][i] = gld16<((st) & 3) * 1024>(hbase, vo[i] + ((st) >> 2) * 4096);                \
    }
#define G2_STEP(st, nwait)                                                                            \
    {                                                                                                 \
        vm_wait8<nwait>(af[(st) % 5], bf[(st) % 5]);                                                  \
        _Pragma("unroll") for (int i = 0; i < 4; ++i)                                                 \
            _Pragma("unroll") for (int j = 0; j < 4; ++j)                                             \
                acc[i][j] = mfma16(af[(st) % 5][i], bf[(st) % 5][j], acc[i][j]);                      \
    }
    G2_LOAD(0) G2_LOAD(1) G2_LOAD(2) G2_LOAD(3) G2_LOAD(4)
    STAMP(3, 1);
    G2_STEP(0, 32)
    STAMP(3, 2);
    if (SIX) {
        G2_LOAD(5)
        G2_STEP(1, 32) G2_STEP(2, 24) G2_STEP(3, 16) G2_STEP(4, 8) G2_STEP(5, 0)
    } else {
        G2_STEP(1, 24) G2_STEP(2, 16) G2_STEP(3, 8) G2_STEP(4, 0)
    }
}

__device__ __forceinline__ float quadsum(float v) {
    {
        const unsigned u = __builtin_bit_cast(unsigned, v);
        auto r = __builtin_amdgcn_permlane16_swap(u, u, false, false);
        v = __builtin_bit_cast(float, (unsigned)r[0]) + __builtin_bit_cast(float, (unsigned)r[1]);
    }
    {
        const unsigned u = __builtin_bit_cast(unsigned, v);
        auto r = __builtin_amdgcn_permlane32_swap(u, u, false, false);
        v = __builtin_bit_cast(float, (unsigned)r[0]) + __builtin_bit_cast(float, (unsigned)r[1]);
    }
    return v;
}

template <bool STATS>
__global__ __launch_bounds__(512, 2) void g2_kernel(const u32x4* __restrict__ hpk, const u32x4* __restrict__ Wd,
                                                    const u32x2* __restrict__ xin, u32x2* __restrict__ xout,
                                                    f32x2* __restrict__ part) {
    __shared__ f32x4 red[8 * 16 * 64 + 128];
    f32x2* sred = reinterpret_cast<f32x2*>(red + 8 * 16 * 64);
    const int b = blockIdx.x;
    const int rb = (b & 7) * 4 + (b >> 6), cb = (b >> 3) & 7;
    const int l = threadIdx.x & 63;
    const int w = __builtin_amdgcn_readfirstlane(threadIdx.x >> 6);
    const int start = 5 * w + (w < 3 ? w : 3);
    const u32x4* hbase = hpk + ((size_t)(rb * 4) * KS2 + start) * 64;
    const u32x4* wbase = Wd + ((size_t)(cb * 4) * KS2 + start) * 64;
    const int n = cb * 64 + 16 * (w >> 1) + 4 * (l >> 4);
    const int s0 = rb * 64 + 32 * (w & 1) + (l & 15);
    const unsigned xo = (unsigned)((n >> 2) * S_ + s0) * 8;
    STAMP(3, 0);
    u32x2 xr0 = gld8<0>(xin, xo), xr1 = gld8<128>(xin, xo);
    f32x4 acc[4][4];
#pragma unroll
    for (int i = 0; i < 4; ++i)
#pragma unroll
        for (int j = 0; j < 4; ++j) acc[i][j] = (f32x4){0.f, 0.f, 0.f, 0.f};
    if (w < 3) g2_main<true>(hbase, wbase, l, acc);
    else g2_main<false>(hbase, wbase, l, acc);
    STAMP(3, 3);
#pragma unroll
    for (int i = 0; i < 4; ++i)
#pragma unroll
        for (int j = 0; j < 4; ++j) red[(w * 16 + i * 4 + j) * 64 + l] = acc[i][j];
    STAMP(3, 4);
    __syncthreads();
    STAMP(3, 5);
    vm_wait2<0>(xr0, xr1);
#pragma unroll
    for (int q = 0; q < 2; ++q) {
        const int tile = 2 * w + q;
        const u32x2 xr = q ? xr1 : xr0;
        f32x4 sum = red[tile * 64 + l];
#pragma unroll
        for (int p = 1; p < 8; ++p) sum += red[(p * 16 + tile) * 64 + l];
        u32x2 o;
        o[0] = pk2(bflo(xr[0]) + rbf(sum[0]), bfhi(xr[0]) + rbf(sum[1]));
        o[1] = pk2(bflo(xr[1]) + rbf(sum[2]), bfhi(xr[1]) + rbf(sum[3]));
        if (STATS) xout[(size_t)(n >> 2) * S_ + s0 + 16 * q] = o;
        else xout[((size_t)(s0 + 16 * q) * D_ + n) >> 2] = o;
        if (STATS) {
            const float v0 = bflo(o[0]), v1 = bfhi(o[0]), v2 = bflo(o[1]), v3 = bfhi(o[1]);
            const float sm = quadsum((v0 + v1) + (v2 + v3));
            const float sq = quadsum((v0 * v0 + v1 * v1) + (v2 * v2 + v3 * v3));
            if (l < 16) { f32x2 e = {sm, sq}; sred[(w >> 1) * 64 + 32 * (w & 1) + 16 * q + l] = e; }
        }
    }
    if (STATS) {
        __syncthreads();
        if (threadIdx.x < 64) {
            const f32x2 e = (sred[threadIdx.x] + sred[64 + threadIdx.x]) + (sred[128 + threadIdx.x] + sred[192 + threadIdx.x]);
            part[(size_t)(rb * 64 + threadIdx.x) * 8 + cb] = e;
        }
    }
    STAMP(3, 6);
}

extern "C" void kernel_launch(void* const* d_in, const int* in_sizes, int n_in,
                              void* d_out, int out_size, void* d_ws, size_t ws_size,
                              hipStream_t stream) {
    const u16* q       = (const u16*)d_in[0];
    const u16* k       = (const u16*)d_in[1];
    const u16* v       = (const u16*)d_in[2];
    const u16* shift_k = (const u16*)d_in[3];
    const u16* ssm_k   = (const u16*)d_in[4];
    const u16* ssm_D   = (const u16*)d_in[5];
    const u16* w_gate  = (const u16*)d_in[7];
    const u16* w_up    = (const u16*)d_in[8];
    const u16* w_down  = (const u16*)d_in[9];
    u16* out = (u16*)d_out;

    char* ws = (char*)d_ws;
    const size_t MB2 = (size_t)S_ * D_ * 2;
    u16*   kvR = (u16*)(ws);
    u16*   Y4  = (u16*)(ws + MB2);
    u16*   x1  = (u16*)(ws + 2 * MB2);
    u16*   x2  = (u16*)(ws + 3 * MB2);
    u16*   x3  = (u16*)(ws + 4 * MB2);
    u32x4* Wgu = (u32x4*)(ws + 5 * MB2);
    u32x4* Wd  = (u32x4*)(ws + 5 * MB2 + (size_t)NFT * KS1 * 2 * 1024);
    u32x4* hpk = (u32x4*)(ws + 5 * MB2 + (size_t)NFT * KS1 * 2 * 1024 + (size_t)32 * KS2 * 1024);

    char* tail = (char*)hpk + (size_t)128 * KS2 * 1024;
    float* csum  = (float*)tail;
    f32x2* part0 = (f32x2*)(tail + 16384);
    f32x2* part2 = (f32x2*)(tail + 16384 + 131072);

    u32x2* x0t = (u32x2*)(tail + 16384 + 2 * 131072);

    prep_kernel<<<256 + PACK_BLOCKS + 86 + 128, 256, 0, stream>>>(k, v, shift_k, w_gate, w_up, w_down, q, kvR, Wgu, Wd,
                                                                  csum, part0, x0t);
    conv_kernel<<<256, 256, 0, stream>>>(kvR, ssm_k, ssm_D, Y4);
    g1_kernel<<<256, 1024, 0, stream>>>(x0t, part0, (const u32x2*)Y4, Wgu, csum, (u32x2*)x1, (u32x2*)hpk);
    g2_kernel<true><<<256, 512, 0, stream>>>(hpk, Wd, (const u32x2*)x1, (u32x2*)x2, part2);
    g1_kernel<<<256, 1024, 0, stream>>>((const u32x2*)x2, part2, (const u32x2*)Y4, Wgu, csum, (u32x2*)x3, (u32x2*)hpk);
    g2_kernel<false><<<256, 512, 0, stream>>>(hpk, Wd, (const u32x2*)x3, (u32x2*)out, part2);
}
```

```cpp
#include <hip/hip_runtime.h>
#include <hip/hip_bf16.h>
#include <stdint.h>

typedef __attribute__((ext_vector_type(8))) __bf16   bf16x8;
typedef __attribute__((ext_vector_type(2))) __bf16   bf16x2;
typedef __attribute__((ext_vector_type(4))) float    f32x4;
typedef __attribute__((ext_vector_type(2))) float    f32x2;
typedef __attribute__((ext_vector_type(4))) unsigned u32x4;
typedef __attribute__((ext_vector_type(2))) unsigned u32x2;
typedef unsigned short u16;

#define STAMP(k, i)

constexpr int S_ = 2048, D_ = 512, F_ = 1376;
constexpr int NFT = 86;
constexpr int KS1 = 16;
constexpr int KS2 = 43;

__device__ __forceinline__ float bflo(unsigned u) { return __builtin_bit_cast(float, u << 16); }
__device__ __forceinline__ float bfhi(unsigned u) { return __builtin_bit_cast(float, u & 0xffff0000u); }
__device__ __forceinline__ unsigned pk2(float lo, float hi) {
    f32x2 v = {lo, hi};
    bf16x2 r = __builtin_convertvector(v, bf16x2);
    return __builtin_bit_cast(unsigned, r);
}
__device__ __forceinline__ float rbf(float x) { return bflo(pk2(x, x)); }
__device__ __forceinline__ f32x4 mfma16(u32x4 a, u32x4 b, f32x4 c) {
    return __builtin_amdgcn_mfma_f32_16x16x32_bf16(__builtin_bit_cast(bf16x8, a), __builtin_bit_cast(bf16x8, b), c, 0, 0, 0);
}

template <int IMM> __device__ __forceinline__ u32x4 gld16(const void* sbase, unsigned voff) {
    u32x4 r;
    asm volatile("global_load_dwordx4 %0, %1, %2 offset:%3" : "=v"(r) : "v"(voff), "s"(sbase), "i"(IMM) : "memory");
    return r;
}
template <int IMM> __device__ __forceinline__ u32x2 gld8(const void* sbase, unsigned voff) {
    u32x2 r;
    asm volatile("global_load_dwordx2 %0, %1, %2 offset:%3" : "=v"(r) : "v"(voff), "s"(sbase), "i"(IMM) : "memory");
    return r;
}
template <int N> __device__ __forceinline__ void vm_wait2(u32x4& a, u32x4& b) {
    asm volatile("s_waitcnt vmcnt(%2)" : "+v"(a), "+v"(b) : "i"(N) : "memory");
}
template <int N> __device__ __forceinline__ void vm_wait8(u32x4 (&a)[4], u32x4 (&b)[4]) {
    asm volatile("s_waitcnt vmcnt(%8)"
                 : "+v"(a[0]), "+v"(a[1]), "+v"(a[2]), "+v"(a[3]), "+v"(b[0]), "+v"(b[1]), "+v"(b[2]), "+v"(b[3])
                 : "i"(N) : "memory");
}
template <int N> __device__ __forceinline__ void vm_wait8(u32x4 (&a)[8]) {
    asm volatile("s_waitcnt vmcnt(%8)"
                 : "+v"(a[0]), "+v"(a[1]), "+v"(a[2]), "+v"(a[3]), "+v"(a[4]), "+v"(a[5]), "+v"(a[6]), "+v"(a[7])
                 : "i"(N) : "memory");
}
template <int N> __device__ __forceinline__ void vm_wait16(u32x2 (&a)[16]) {
    asm volatile("s_waitcnt vmcnt(%16)"
                 : "+v"(a[0]), "+v"(a[1]), "+v"(a[2]), "+v"(a[3]), "+v"(a[4]), "+v"(a[5]), "+v"(a[6]), "+v"(a[7]),
                   "+v"(a[8]), "+v"(a[9]), "+v"(a[10]), "+v"(a[11]), "+v"(a[12]), "+v"(a[13]), "+v"(a[14]), "+v"(a[15])
                 : "i"(N) : "memory");
}
template <int N> __device__ __forceinline__ void vm_wait1(u32x2& a) {
    asm volatile("s_waitcnt vmcnt(%1)" : "+v"(a) : "i"(N) : "memory");
}
template <int N> __device__ __forceinline__ void vm_wait2(u32x2& a, u32x2& b) {
    asm volatile("s_waitcnt vmcnt(%2)" : "+v"(a), "+v"(b) : "i"(N) : "memory");
}

constexpr int PACK_GU_THREADS = 2 * 64 * 688;
constexpr int PACK_D_THREADS  = 172 * 256;
constexpr int PACK_BLOCKS = (PACK_GU_THREADS + PACK_D_THREADS) / 256;

__device__ __forceinline__ void pack_item(const u16* __restrict__ src, int N, int ko, int np, u32x4* __restrict__ dst0) {
    unsigned w[8];
#pragma unroll
    for (int j = 0; j < 8; ++j) w[j] = *reinterpret_cast<const unsigned*>(src + (size_t)(8 * ko + j) * N + 2 * np);
    u32x4 o0, o1;
#pragma unroll
    for (int c = 0; c < 4; ++c) {
        o0[c] = (w[2 * c] & 0xffffu) | (w[2 * c + 1] << 16);
        o1[c] = (w[2 * c] >> 16) | (w[2 * c + 1] & 0xffff0000u);
    }
    dst0[0] = o0;
    dst0[1] = o1;
}

__global__ __launch_bounds__(256) void prep_kernel(const u16* __restrict__ k, const u16* __restrict__ v,
                                                   const u16* __restrict__ shift_k,
                                                   const u16* __restrict__ wg, const u16* __restrict__ wu,
                                                   const u16* __restrict__ wd,
                                                   const u16* __restrict__ x0,
                                                   u16* __restrict__ kvR, u32x4* __restrict__ Wgu,
                                                   u32x4* __restrict__ Wd, float* __restrict__ csum,
                                                   f32x2* __restrict__ part0, u32x2* __restrict__ x0t) {
    __shared__ unsigned tile[64 * 33];
    const int t = threadIdx.x;
    STAMP(0, 0);
    if (blockIdx.x < 256) {
        const int st = blockIdx.x >> 3, dt = blockIdx.x & 7;
        const int s0 = st * 64, d0 = dt * 64;
        const int c8 = t & 7, r = t >> 3;
        const int sA = s0 + 2 * r;
        const int dd = d0 + 8 * c8;
        u32x4 kr[5], vr[2], wv[4];
#pragma unroll
        for (int i = 0; i < 5; ++i) {
            const int s = sA - 3 + i;
            u32x4 z = {0u, 0u, 0u, 0u};
            kr[i] = (s >= 0) ? *reinterpret_cast<const u32x4*>(k + (size_t)s * D_ + dd) : z;
        }
#pragma unroll
        for (int i = 0; i < 2; ++i) vr[i] = *reinterpret_cast<const u32x4*>(v + (size_t)(sA + i) * D_ + dd);
#pragma unroll
        for (int i = 0; i < 4; ++i) wv[i] = *reinterpret_cast<const u32x4*>(shift_k + (size_t)dd * 4 + 8 * i);
#pragma unroll
        for (int j = 0; j < 8; ++j) {
            const unsigned wa = wv[j >> 1][(j & 1) * 2], wb = wv[j >> 1][(j & 1) * 2 + 1];
            const float w0 = bflo(wa), w1 = bfhi(wa), w2 = bflo(wb), w3 = bfhi(wb);
            float kk[5];
#pragma unroll
            for (int i = 0; i < 5; ++i) kk[i] = (j & 1) ? bfhi(kr[i][j >> 1]) : bflo(kr[i][j >> 1]);
            float res[2];
#pragma unroll
            for (int q = 0; q < 2; ++q) {
                float acc = rbf(kk[q] * w3);
                acc = rbf(acc + rbf(kk[q + 1] * w2));
                acc = rbf(acc + rbf(kk[q + 2] * w1));
                acc = rbf(acc + rbf(kk[q + 3] * w0));
                const float vv = (j & 1) ? bfhi(vr[q][j >> 1]) : bflo(vr[q][j >> 1]);
                res[q] = acc * vv;
            }
            tile[(8 * c8 + j) * 33 + (31 - r)] = pk2(res[1], res[0]);
        }
        STAMP(0, 1);
        __syncthreads();
#pragma unroll
        for (int i = 0; i < 2; ++i) {
            const int idx = t + 256 * i;
            const int d = idx >> 3, q = idx & 7;
            u32x4 o;
#pragma unroll
            for (int c = 0; c < 4; ++c) o[c] = tile[d * 33 + 4 * q + c];
            *reinterpret_cast<u32x4*>(kvR + (size_t)(d0 + d) * S_ + (S_ - 64 - s0) + 8 * q) = o;
        }
    } else if (blockIdx.x >= 256 + PACK_BLOCKS + 86) {
        const int r0 = (blockIdx.x - (256 + PACK_BLOCKS + 86)) * 16;
        const int row = t & 15;
        f32x2* sp = reinterpret_cast<f32x2*>(tile);
        u32x4 a[4];
#pragma unroll
        for (int i = 0; i < 4; ++i) a[i] = *reinterpret_cast<const u32x4*>(x0 + (size_t)(r0 + row) * D_ + 8 * ((t >> 4) + 16 * i));
#pragma unroll
        for (int i = 0; i < 4; ++i) {
            const int o = (t >> 4) + 16 * i;
            u32x2 lo = {a[i][0], a[i][1]}, hi = {a[i][2], a[i][3]};
            x0t[(size_t)(2 * o) * S_ + r0 + row] = lo;
            x0t[(size_t)(2 * o + 1) * S_ + r0 + row] = hi;
            float sm = 0.f, sq = 0.f;
#pragma unroll
            for (int j = 0; j < 4; ++j) {
                const float l0 = bflo(a[i][j]), h0 = bfhi(a[i][j]);
                sm += l0 + h0;
                sq += l0 * l0 + h0 * h0;
            }
            f32x2 e = {sm, sq};
            sp[row * 64 + o] = e;
        }
        __syncthreads();
        if (t < 128) {
            const int rr = t >> 3, g = t & 7;
            f32x2 e = sp[rr * 64 + 8 * g];
#pragma unroll
            for (int j = 1; j < 8; ++j) e += sp[rr * 64 + 8 * g + j];
            part0[(size_t)(r0 + rr) * 8 + g] = e;
        }
    } else if (blockIdx.x >= 256 + PACK_BLOCKS) {
        const int cbk = blockIdx.x - (256 + PACK_BLOCKS);
        const int m = cbk / 43, f0 = (cbk % 43) * 32;
        const u16* src = m ? wu : wg;
        const int np = t & 15, kg = t >> 4;
        unsigned wv[32];
#pragma unroll
        for (int j = 0; j < 32; ++j) wv[j] = *reinterpret_cast<const unsigned*>(src + (size_t)(32 * kg + j) * F_ + f0 + 2 * np);
        float s0 = 0.f, s1 = 0.f;
#pragma unroll
        for (int j = 0; j < 32; ++j) { s0 += bflo(wv[j]); s1 += bfhi(wv[j]); }
        float* red = reinterpret_cast<float*>(tile);
        red[kg * 33 + 2 * np] = s0;
        red[kg * 33 + 2 * np + 1] = s1;
        __syncthreads();
        if (t < 32) {
            float a = 0.f;
#pragma unroll
            for (int j = 0; j < 16; ++j) a += red[j * 33 + t];
            csum[m * F_ + f0 + t] = a;
        }
    } else {
        int id = (blockIdx.x - 256) * 256 + t;
        if (id < PACK_GU_THREADS) {
            const int np = id % 688;
            const int rest = id / 688;
            const int ko = rest & 63, m = rest >> 6;
            const int n = 2 * np;
            const int ft = n >> 4, ks = ko >> 2, g = ko & 3;
            pack_item(m ? wu : wg, F_, ko, np, Wgu + ((size_t)((ft * KS1 + ks) * 2 + m) * 64 + g * 16 + (n & 15)));
        } else {
            id -= PACK_GU_THREADS;
            const int np = id & 255;
            const int ko = id >> 8;
            const int n = 2 * np;
            const int nt = n >> 4, ks = ko >> 2, g = ko & 3;
            pack_item(wd, D_, ko, np, Wd + ((size_t)(nt * KS2 + ks) * 64 + g * 16 + (n & 15)));
        }
    }
    STAMP(0, 2);
}

constexpr int CV_C = 4160;
constexpr int CV_U = 4 * CV_C;
constexpr int CV_CH = CV_U + 4608;
constexpr int CV_Y = 4 * CV_CH;

template <int OFF> __device__ __forceinline__ u32x2 lds8(unsigned addr) {
    u32x2 r;
    asm volatile("ds_read_b64 %0, %1 offset:%2" : "=v"(r) : "v"(addr), "i"(OFF) : "memory");
    return r;
}
template <int OFF> __device__ __forceinline__ u32x4 lds16(unsigned addr) {
    u32x4 r;
    asm volatile("ds_read_b128 %0, %1 offset:%2" : "=v"(r) : "v"(addr), "i"(OFF) : "memory");
    return r;
}
__device__ __forceinline__ void lgkm_wait(u32x2 (&a)[16], u32x4 (&b)[8]) {
    asm volatile("s_waitcnt lgkmcnt(0)"
                 : "+v"(a[0]), "+v"(a[1]), "+v"(a[2]), "+v"(a[3]), "+v"(a[4]), "+v"(a[5]), "+v"(a[6]), "+v"(a[7]),
                   "+v"(a[8]), "+v"(a[9]), "+v"(a[10]), "+v"(a[11]), "+v"(a[12]), "+v"(a[13]), "+v"(a[14]), "+v"(a[15]),
                   "+v"(b[0]), "+v"(b[1]), "+v"(b[2]), "+v"(b[3]), "+v"(b[4]), "+v"(b[5]), "+v"(b[6]), "+v"(b[7])
                 :: "memory");
}
template <int BLK, int GBLK>
__device__ __forceinline__ void conv_load(unsigned fa, unsigned ga, u32x2 (&F)[16], u32x4 (&G)[8]) {
#define CVL(i) F[2 * (i)] = lds8<64 * (8 * BLK + (i))>(fa); F[2 * (i) + 1] = lds8<64 * (8 * BLK + (i)) + 8>(fa); \
               G[i] = lds16<64 * (8 * (GBLK + 7) + (i))>(ga);
    CVL(0) CVL(1) CVL(2) CVL(3) CVL(4) CVL(5) CVL(6) CVL(7)
#undef CVL
}
__device__ __forceinline__ u32x4 cat(u32x2 a, u32x2 b) { u32x4 r = {a[0], a[1], b[0], b[1]}; return r; }

template <int N, int T>
__device__ __forceinline__ void conv_step(unsigned fa, unsigned ga, u32x2 (&Fr)[3][16], u32x4 (&Gr)[2][8], f32x4& acc1,
                                          f32x4& acc0) {
    if (T >= 0) lgkm_wait(Fr[T % 3], Gr[T & 1]);
    if (T + 1 <= N + 1) conv_load<T + 1, T - N>(fa, ga, Fr[(T + 1) % 3], Gr[(T + 1) & 1]);
    __builtin_amdgcn_sched_barrier(0);
    if (T >= 0) {
#pragma unroll
        for (int i = 0; i < 8; ++i) {
            acc1 = mfma16(cat(Fr[T % 3][2 * i], Fr[T % 3][2 * i + 1]), Gr[T & 1][i], acc1);
            if (T >= 1) acc0 = mfma16(cat(Fr[(T + 2) % 3][2 * i], Fr[(T + 2) % 3][2 * i + 1]), Gr[T & 1][i], acc0);
        }
    }
    __builtin_amdgcn_sched_barrier(0);
    if constexpr (T < N + 1) conv_step<N, T + 1>(fa, ga, Fr, Gr, acc1, acc0);
}
template <int N>
__device__ __forceinline__ void conv_task(unsigned fa, unsigned ga, f32x4& acc1, f32x4& acc0) {
    u32x2 Fr[3][16];
    u32x4 Gr[2][8];
    conv_step<N, -1>(fa, ga, Fr, Gr, acc1, acc0);
}

__device__ __forceinline__ unsigned sh16(unsigned lo, unsigned hi) { return (lo >> 16) | (hi << 16); }

__global__ __launch_bounds__(256) void conv_kernel(const u16* __restrict__ kvR, const u16* __restrict__ ssm_k,
                                                   const u16* __restrict__ ssm_D, u16* __restrict__ Y4) {
    __shared__ __attribute__((aligned(16))) char smem[CV_Y + 8704];
    const int t = threadIdx.x, l = t & 63;
    const int w = __builtin_amdgcn_readfirstlane(t >> 6);
    const int cq = blockIdx.x >> 1, half = blockIdx.x & 1;
    const int ch = 4 * cq + w;
    char* base = smem + w * CV_CH;
    STAMP(1, 0);
    const float Dv = bflo((unsigned)ssm_D[ch]);
    {
        const u16* hp = ssm_k + (size_t)ch * S_;
        const u16* up = kvR + (size_t)ch * S_;
        u32x4 hv[4], uv[4];
        u32x2 hx[4];
#pragma unroll
        for (int i = 0; i < 4; ++i) {
            const int m = l + 64 * i;
            hv[i] = *reinterpret_cast<const u32x4*>(hp + 8 * m);
            uv[i] = *reinterpret_cast<const u32x4*>(up + 8 * m);
            const u32x2 zz = {0u, 0u};
            hx[i] = (m < 255) ? *reinterpret_cast<const u32x2*>(hp + 8 * m + 8) : zz;
        }
        const u32x4 z = {0u, 0u, 0u, 0u};
        if (l < 32) *reinterpret_cast<u32x4*>(base + CV_U + 4096 + 16 * l) = z;
        if (l < 4) *reinterpret_cast<u32x4*>(base + l * CV_C) = z;
        if (l == 0) {
            const unsigned h01 = hv[0][0], h23 = hv[0][1];
            u32x4 c0 = z, c1 = z, c2 = z, c3 = z;
            c0[3] = h01 << 16;
            c1[3] = h01;
            c2[2] = h01 << 16; c2[3] = sh16(h01, h23);
            c3[2] = h01;       c3[3] = h23;
            *reinterpret_cast<u32x4*>(base + 0 * CV_C + 16) = c0;
            *reinterpret_cast<u32x4*>(base + 1 * CV_C + 16) = c1;
            *reinterpret_cast<u32x4*>(base + 2 * CV_C + 16) = c2;
            *reinterpret_cast<u32x4*>(base + 3 * CV_C + 16) = c3;
        }
#pragma unroll
        for (int i = 0; i < 4; ++i) {
            const int m = l + 64 * i;
            const unsigned d0 = hv[i][0], d1 = hv[i][1], d2 = hv[i][2], d3 = hv[i][3], d4 = hx[i][0], d5 = hx[i][1];
            const unsigned e0 = sh16(d0, d1), e1 = sh16(d1, d2), e2 = sh16(d2, d3), e3 = sh16(d3, d4), e4 = sh16(d4, d5);
            u32x4 c0 = {e0, e1, e2, e3}, c1 = {d1, d2, d3, d4}, c2 = {e1, e2, e3, e4}, c3 = {d2, d3, d4, d5};
            *reinterpret_cast<u32x4*>(base + 0 * CV_C + 32 + 16 * m) = c0;
            *reinterpret_cast<u32x4*>(base + 1 * CV_C + 32 + 16 * m) = c1;
            *reinterpret_cast<u32x4*>(base + 2 * CV_C + 32 + 16 * m) = c2;
            *reinterpret_cast<u32x4*>(base + 3 * CV_C + 32 + 16 * m) = c3;
            *reinterpret_cast<u32x4*>(base + CV_U + 16 * m) = uv[i];
        }
    }
    STAMP(1, 1);
    __syncthreads();
    STAMP(1, 2);

    const int o0 = (l & 15) + 8 * (l >> 4);
    const unsigned fp = (unsigned)(size_t)(base + (l & 3) * CV_C + 2 * (o0 & ~3));
    const unsigned gp = (unsigned)(size_t)(base + CV_U + 2 * (2032 - 16 * (l & 15) + 8 * (l >> 4)) - 64 * 56);
    f32x4 a0 = {0.f, 0.f, 0.f, 0.f}, a1 = a0, a2 = a0, a3 = a0;
    int n0, n1, n2, n3;
    if (half == 0) {
        n0 = 7; n1 = 6; n2 = 1; n3 = 0;
        conv_task<6>(fp, gp, a0, a1);
        conv_task<0>(fp, gp, a2, a3);
    } else {
        n0 = 5; n1 = 4; n2 = 3; n3 = 2;
        conv_task<4>(fp, gp, a0, a1);
        conv_task<2>(fp, gp, a2, a3);
    }
    STAMP(1, 3);
    u16* yb = reinterpret_cast<u16*>(smem + CV_Y);
    const int sl = 16 * (l & 15) + 4 * (l >> 4);
    const int slp = sl + (l & 15);
#define EPI(acc, n, i)                                                                        \
    {                                                                                         \
        const int s = 256 * (n) + sl;                                                         \
        const u32x2 uu = *reinterpret_cast<const u32x2*>(base + CV_U + 2 * (2044 - s));       \
        const float u0 = bfhi(uu[1]), u1 = bflo(uu[1]), u2 = bfhi(uu[0]), u3 = bflo(uu[0]);   \
        const unsigned c01 = pk2(acc[0], acc[1]), c23 = pk2(acc[2], acc[3]);                  \
        const unsigned k01 = pk2(u0 * Dv, u1 * Dv), k23 = pk2(u2 * Dv, u3 * Dv);              \
        const unsigned p01 = pk2(bflo(c01) + bflo(k01), bfhi(c01) + bfhi(k01));               \
        const unsigned p23 = pk2(bflo(c23) + bflo(k23), bfhi(c23) + bfhi(k23));               \
        u16* q = yb + ((i) * 272 + slp) * 4 + w;                                              \
        q[0] = (u16)p01; q[4] = (u16)(p01 >> 16); q[8] = (u16)p23; q[12] = (u16)(p23 >> 16);  \
    }
    EPI(a0, n0, 0) EPI(a1, n1, 1) EPI(a2, n2, 2) EPI(a3, n3, 3)
    __syncthreads();
    {
        const u32x2* yr = reinterpret_cast<const u32x2*>(smem + CV_Y) + t + (t >> 4);
        u32x2* dst = reinterpret_cast<u32x2*>(Y4) + (size_t)cq * S_;
        dst[256 * n0 + t] = yr[0];
        dst[256 * n1 + t] = yr[272];
        dst[256 * n2 + t] = yr[544];
        dst[256 * n3 + t] = yr[816];
    }
    STAMP(1, 4);
}

__device__ __forceinline__ float rowsum8(float v) {
    v += __builtin_bit_cast(float, __builtin_amdgcn_update_dpp(0, __builtin_bit_cast(int, v), 0x128, 0xf, 0xf, false));
    {
        const unsigned u = __builtin_bit_cast(unsigned, v);
        auto r = __builtin_amdgcn_permlane16_swap(u, u, false, false);
        v = __builtin_bit_cast(float, (unsigned)r[0]) + __builtin_bit_cast(float, (unsigned)r[1]);
    }
    {
        const unsigned u = __builtin_bit_cast(unsigned, v);
        auto r = __builtin_amdgcn_permlane32_swap(u, u, false, false);
        v = __builtin_bit_cast(float, (unsigned)r[0]) + __builtin_bit_cast(float, (unsigned)r[1]);
    }
    return v;
}
__device__ __forceinline__ f32x2 unpk(unsigned u) { f32x2 r = {bflo(u), bfhi(u)}; return r; }

__device__ __forceinline__ void g1_epilogue(const f32x4 (&ag)[4], const f32x4 (&au)[4], int rb, int ft, int l,
                                            u32x2* __restrict__ hpk, const f32x2* st2, u32x4 cgu, u32x4 cuu) {
    const int ks2 = ft >> 1;
    const int slot = (2 * (ft & 1) + (l >> 5)) * 16 + (l & 15);
    const f32x4 cg = __builtin_bit_cast(f32x4, cgu), cu = __builtin_bit_cast(f32x4, cuu);
    float sa[4], sb[4];
#pragma unroll
    for (int rt = 0; rt < 4; ++rt) {
        const f32x4 pp = *reinterpret_cast<const f32x4*>(st2 + 2 * (16 * rt + (l & 15)));
        const float mean2 = (pp[0] + pp[2]) * (1.0f / 512.0f);
        const float inv2 = __builtin_amdgcn_rsqf(fmaxf((pp[1] + pp[3]) * (1.0f / 512.0f) - mean2 * mean2, 0.f) + 1e-5f);
        sa[rt] = inv2;
        sb[rt] = -mean2 * inv2;
    }
    u32x2* dst = hpk + ((size_t)(rb * 4 * KS2 + ks2) * 64 + slot) * 2 + ((l >> 4) & 1);
#pragma unroll
    for (int rt = 0; rt < 4; ++rt) {
        const f32x4 g4 = ag[rt] * sa[rt] + cg * sb[rt];
        const f32x4 u4 = au[rt] * sa[rt] + cu * sb[rt];
        const unsigned g01 = pk2(g4[0], g4[1]), g23 = pk2(g4[2], g4[3]);
        const unsigned u01 = pk2(u4[0], u4[1]), u23 = pk2(u4[2], u4[3]);
        const float g0 = bflo(g01), g1 = bfhi(g01), g2 = bflo(g23), g3 = bfhi(g23);
        const float e0 = __expf(-g0), e1 = __expf(-g1), e2 = __expf(-g2), e3 = __expf(-g3);
        const float r0 = __builtin_amdgcn_rcpf(1.0f + e0), r1 = __builtin_amdgcn_rcpf(1.0f + e1);
        const float r2 = __builtin_amdgcn_rcpf(1.0f + e2), r3 = __builtin_amdgcn_rcpf(1.0f + e3);
        const unsigned s01 = pk2(g0 * r0, g1 * r1), s23 = pk2(g2 * r2, g3 * r3);
        u32x2 o;
        o[0] = pk2(bflo(s01) * bflo(u01), bfhi(s01) * bfhi(u01));
        o[1] = pk2(bflo(s23) * bflo(u23), bfhi(s23) * bfhi(u23));
        dst[(size_t)rt * KS2 * 128] = o;
    }
}

constexpr int G1_WIN = 6;
constexpr int G1_NL = 0;
__device__ constexpr bool g1_is_reg(int t) { return t < G1_WIN || t >= G1_WIN + G1_NL; }
__device__ constexpr int g1_ridx(int t) { return t < G1_WIN ? t : t - G1_NL; }
__device__ constexpr int g1_rstep(int r) { return r < G1_WIN ? r : r + G1_NL; }
__device__ constexpr int g1_younger(int t, int T) {
    int pos[40] = {};
    int n = 2;
    for (int u = 0; u < G1_WIN + G1_NL; ++u) { n += 2; pos[u] = n; }
    for (int u = 0; u < T; ++u) {
        if (u == t) return n - pos[t];
        if (g1_is_reg(u)) {
            const int nx = g1_rstep(g1_ridx(u) + G1_WIN);
            if (nx < T) { n += 2; pos[nx] = n; }
        }
        if (u == 15 && T > 16) n += 4;
    }
    return 0;
}
__device__ constexpr int g1_ks(int t) { return ((t & 15) < 4) ? (t & 15) : ((t & 15) < 8) ? (t & 15) + 4 : ((t & 15) < 12) ? (t & 15) - 4 : (t & 15); }
#define G1_WPTR(t) ((t) < 16 ? wbase : wbase2)
#define G1_LOADW(t)                                                                                                   \
    {                                                                                                                 \
        wf[g1_ridx(t) % G1_WIN][0] = gld16<((2 * g1_ks(t)) & 3) * 1024>(G1_WPTR(t), wo + ((2 * g1_ks(t)) >> 2) * 4096);     \
        wf[g1_ridx(t) % G1_WIN][1] = gld16<((2 * g1_ks(t) + 1) & 3) * 1024>(G1_WPTR(t), wo + ((2 * g1_ks(t)) >> 2) * 4096); \
    }
#define G1_STEP(t, T)                                                                         \
    {                                                                                         \
        u32x4 w0, w1;                                                                         \
        if (g1_is_reg(t)) {                                                                   \
            vm_wait2<g1_younger(t, T)>(wf[g1_ridx(t) % G1_WIN][0], wf[g1_ridx(t) % G1_WIN][1]); \
            w0 = wf[g1_ridx(t) % G1_WIN][0];                                                  \
            w1 = wf[g1_ridx(t) % G1_WIN][1];                                                  \
        } else {                                                                              \
            asm volatile("s_waitcnt vmcnt(%0)" :: "i"(g1_younger(t, T)) : "memory");          \
            w0 = wl[(2 * ((t) - G1_WIN)) * 64 + l];                                           \
            w1 = wl[(2 * ((t) - G1_WIN) + 1) * 64 + l];                                       \
        }                                                                                     \
        _Pragma("unroll") for (int rt = 0; rt < 4; ++rt) {                                    \
            const u32x4 bb = At[(rt * KS1 + g1_ks(t)) * 64 + l];                            \
            ag[rt] = mfma16(w0, bb, ag[rt]);                                                  \
            au[rt] = mfma16(w1, bb, au[rt]);                                                  \
        }                                                                                     \
        if (g1_is_reg(t) && g1_rstep(g1_ridx(t) + G1_WIN) < (T)) G1_LOADW(g1_rstep(g1_ridx(t) + G1_WIN)) \
    }
#define G1_STEPS8(b, T)                                                                                           \
    G1_STEP(b + 0, T) G1_STEP(b + 1, T) G1_STEP(b + 2, T) G1_STEP(b + 3, T) G1_STEP(b + 4, T) G1_STEP(b + 5, T)   \
    G1_STEP(b + 6, T) G1_STEP(b + 7, T)

__device__ __forceinline__ void dma16(const void* gsrc, unsigned lds_dst) {
    unsigned keep;
    asm volatile("s_mov_b32 %0, m0\n\ts_mov_b32 m0, %2\n\ts_nop 0\n\tglobal_load_lds_dwordx4 %1, off\n\ts_mov_b32 m0, %0"
                 : "=&s"(keep) : "v"(gsrc), "s"(lds_dst) : "memory");
}

template <bool LATE>
__device__ __forceinline__ void g1_mfma_role(const u32x4* At, const f32x2* st2, u32x4* wl, const u32x4* wbase,
                                             const float* csum, int l, int rb, int ft, u32x2* __restrict__ hpk) {
    constexpr int T = 16;
    const u32x4* wbase2 = wbase;
    const unsigned wo = 16 * l;
    u32x4 wf[G1_WIN][2];
    const unsigned co = (16 * ft + 4 * (l >> 4)) * 4;
    u32x4 cs[2];
    cs[0] = gld16<0>(csum, co);
    cs[1] = gld16<0>(csum, co + F_ * 4);
    G1_LOADW(0) G1_LOADW(1) G1_LOADW(2) G1_LOADW(3)
    STAMP(2, 4);
    __syncthreads();
    STAMP(2, 5);
    G1_LOADW(4)
    if (G1_WIN > 5) G1_LOADW(5)
    if (G1_WIN > 6) G1_LOADW(6)
    if (G1_WIN > 7) G1_LOADW(7)
    if (G1_WIN > 8) G1_LOADW(8)
    f32x4 ag[4], au[4];
#pragma unroll
    for (int r = 0; r < 4; ++r) { ag[r] = (f32x4){0.f, 0.f, 0.f, 0.f}; au[r] = ag[r]; }
    G1_STEPS8(0, T)
    if (!LATE) __syncthreads();
    G1_STEPS8(8, T)
    vm_wait2<0>(cs[0], cs[1]);
    STAMP(2, 6);
    g1_epilogue(ag, au, rb, ft, l, hpk, st2, cs[0], cs[1]);
}

__global__ __launch_bounds__(1024, 4) void g1_kernel(const u32x2* __restrict__ x, const f32x2* __restrict__ part,
                                                     const u32x2* __restrict__ Y4, const u32x4* __restrict__ Wgu,
                                                     const float* __restrict__ csum, u32x2* __restrict__ x1out,
                                                     u32x2* __restrict__ hpk) {
    __shared__ u32x4 At[4 * KS1 * 64 + 64 + 8 * 2 * G1_NL * 64];
    f32x2* st2 = reinterpret_cast<f32x2*>(At + 4 * KS1 * 64);
    const int b = blockIdx.x;
    const int rb = (b & 7) * 4 + (b >> 6), cb = (b >> 3) & 7;
    const int l = threadIdx.x & 63;
    const int w = __builtin_amdgcn_readfirstlane(threadIdx.x >> 6);
    const int ntl = cb < 6 ? 11 : 10;
    const int ft0 = cb < 6 ? 11 * cb : 66 + 10 * (cb - 6);
    STAMP(2, 0);
    if (w < 8) {
        const int ip = l & 7, g = l >> 3;
        const int rt = w >> 1, dh = w & 1;
        const int row = rb * 64 + 16 * rt + 2 * ip;
        u32x4 xv[8], yv[8];
        u32x2 pa = gld8<0>(part, (unsigned)(row * 8 + g) * 8);
        u32x2 pb = gld8<64>(part, (unsigned)(row * 8 + g) * 8);
        const unsigned o0 = (unsigned)((64 * dh + g) * S_ + row) * 8;
#pragma unroll
        for (int c = 0; c < 8; ++c) {
            xv[c] = gld16<0>(x, o0 + c * (8 * S_ * 8));
            yv[c] = gld16<0>(Y4, o0 + c * (8 * S_ * 8));
        }
        STAMP(2, 5);
        vm_wait2<16>(pa, pb);
        const float meanA = rowsum8(__builtin_bit_cast(float, (unsigned)pa[0])) * (1.0f / 512.0f);
        const float msqA = rowsum8(__builtin_bit_cast(float, (unsigned)pa[1])) * (1.0f / 512.0f);
        const float invA = 1.0f / sqrtf(fmaxf(msqA - meanA * meanA, 0.f) + 1e-5f);
        const float meanB = rowsum8(__builtin_bit_cast(float, (unsigned)pb[0])) * (1.0f / 512.0f);
        const float msqB = rowsum8(__builtin_bit_cast(float, (unsigned)pb[1])) * (1.0f / 512.0f);
        const float invB = 1.0f / sqrtf(fmaxf(msqB - meanB * meanB, 0.f) + 1e-5f);
        const f32x2 iA = {invA, invA}, bA = {-meanA * invA, -meanA * invA};
        const f32x2 iB = {invB, invB}, bB = {-meanB * invB, -meanB * invB};
        f32x2 sa = {0.f, 0.f}, sb = sa, qa = sa, qb = sa;
        u32x2* At2 = reinterpret_cast<u32x2*>(At);
#define G1_TRANS(c)                                                                                   \
        {                                                                                             \
            vm_wait2<14 - 2 * (c)>(xv[c], yv[c]);                                                     \
            u32x4 o;                                                                                  \
            _Pragma("unroll") for (int j = 0; j < 4; ++j) {                                           \
                const f32x2 xx = unpk(xv[c][j]);                                                      \
                const f32x2 yy = unpk(yv[c][j]);                                                      \
                const f32x2 q = (j < 2) ? (xx * iA + bA) : (xx * iB + bB);                            \
                const f32x2 r = xx + q * yy;                                                          \
                o[j] = pk2(r[0], r[1]);                                                               \
                if (j < 2) { sa += r; qa += r * r; } else { sb += r; qb += r * r; }                   \
            }                                                                                         \
            const int unit = (rt * KS1 + 8 * dh + (c)) * 64 + (g >> 1) * 16 + 2 * ip;                 \
            u32x2 oa = {o[0], o[1]}, ob = {o[2], o[3]};                                               \
            At2[2 * unit + (g & 1)] = oa;                                                             \
            At2[2 * (unit + 1) + (g & 1)] = ob;                                                       \
            xv[c] = o;                                                                                \
        }
        G1_TRANS(0) G1_TRANS(1) G1_TRANS(2) G1_TRANS(3)
        STAMP(2, 1);
        __syncthreads();
        STAMP(2, 3);
        G1_TRANS(4) G1_TRANS(5) G1_TRANS(6) G1_TRANS(7)
#pragma unroll
        for (int c = 0; c < 8; ++c)
            if ((8 * dh + c) >> 1 == cb)
                *reinterpret_cast<u32x4*>(x1out + (size_t)(64 * dh + 8 * c + g) * S_ + row) = xv[c];
        const float s_a = rowsum8(sa[0] + sa[1]), q_a = rowsum8(qa[0] + qa[1]);
        const float s_b = rowsum8(sb[0] + sb[1]), q_b = rowsum8(qb[0] + qb[1]);
        if (g == 0) {
            f32x2 ea = {s_a, q_a}, eb = {s_b, q_b};
            st2[2 * (16 * rt + 2 * ip) + dh] = ea;
            st2[2 * (16 * rt + 2 * ip + 1) + dh] = eb;
        }
        STAMP(2, 4);
        if (w + 8 < ntl) g1_mfma_role<true>(At, st2, At, Wgu + (size_t)(ft0 + 8 + w) * (KS1 * 2 * 64), csum, l, rb, ft0 + 8 + w, hpk);
        else __syncthreads();
    } else {
        g1_mfma_role<false>(At, st2, At, Wgu + (size_t)(ft0 + w - 8) * (KS1 * 2 * 64), csum, l, rb, ft0 + w - 8, hpk);
    }
    STAMP(2, 7);
}

__device__ __forceinline__ void g2_main(const u32x4* hbase, const u32x4* wbase, int l, bool six, f32x4 (&acc)[4][4]) {
    u32x4 af[5][4], bf[5][4];
    unsigned vo[4];
#pragma unroll
    for (int i = 0; i < 4; ++i) vo[i] = 16 * l + i * (KS2 * 1024);
#define G2_LOAD(st)                                                                                   \
    _Pragma("unroll") for (int i = 0; i < 4; ++i) {                                                   \
        af[(st) % 5][i] = gld16<((st) & 3) * 1024>(wbase, vo[i] + ((st) >> 2) * 4096);                \
        bf[(st) % 5][i] = gld16<((st) & 3) * 1024>(hbase, vo[i] + ((st) >> 2) * 4096);                \
    }
#define G2_STEP(st, nwait)                                                                            \
    {                                                                                                 \
        vm_wait8<nwait>(af[(st) % 5], bf[(st) % 5]);                                                  \
        _Pragma("unroll") for (int i = 0; i < 4; ++i)                                                 \
            _Pragma("unroll") for (int j = 0; j < 4; ++j)                                             \
                acc[i][j] = mfma16(af[(st) % 5][i], bf[(st) % 5][j], acc[i][j]);                      \
        __builtin_amdgcn_sched_barrier(0);                                                            \
    }
    G2_LOAD(0) G2_LOAD(1) G2_LOAD(2) G2_LOAD(3) G2_LOAD(4)
    STAMP(3, 1);
    G2_STEP(0, 32)
    STAMP(3, 2);
    if (six) { G2_LOAD(5) }
    G2_STEP(1, 24) G2_STEP(2, 16) G2_STEP(3, 8) G2_STEP(4, 0)
    if (six) { G2_STEP(5, 0) }
}

__device__ __forceinline__ float quadsum(float v) {
    {
        const unsigned u = __builtin_bit_cast(unsigned, v);
        auto r = __builtin_amdgcn_permlane16_swap(u, u, false, false);
        v = __builtin_bit_cast(float, (unsigned)r[0]) + __builtin_bit_cast(float, (unsigned)r[1]);
    }
    {
        const unsigned u = __builtin_bit_cast(unsigned, v);
        auto r = __builtin_amdgcn_permlane32_swap(u, u, false, false);
        v = __builtin_bit_cast(float, (unsigned)r[0]) + __builtin_bit_cast(float, (unsigned)r[1]);
    }
    return v;
}

__global__ __launch_bounds__(512, 2) void g2_kernel(const u32x4* __restrict__ hpk, const u32x4* __restrict__ Wd,
                                                    const u32x2* __restrict__ xin, u32x2* __restrict__ xout,
                                                    f32x2* __restrict__ part, int STATS) {
    __shared__ f32x4 red[8 * 16 * 64 + 128];
    f32x2* sred = reinterpret_cast<f32x2*>(red + 8 * 16 * 64);
    const int b = blockIdx.x;
    const int rb = (b & 7) * 4 + (b >> 6), cb = (b >> 3) & 7;
    const int l = threadIdx.x & 63;
    const int w = __builtin_amdgcn_readfirstlane(threadIdx.x >> 6);
    const int start = 5 * w + (w < 3 ? w : 3);
    const u32x4* hbase = hpk + ((size_t)(rb * 4) * KS2 + start) * 64;
    const u32x4* wbase = Wd + ((size_t)(cb * 4) * KS2 + start) * 64;
    const int n = cb * 64 + 16 * (w >> 1) + 4 * (l >> 4);
    const int s0 = rb * 64 + 32 * (w & 1) + (l & 15);
    const unsigned xo = (unsigned)((n >> 2) * S_ + s0) * 8;
    STAMP(3, 0);
    u32x2 xr0 = gld8<0>(xin, xo), xr1 = gld8<128>(xin, xo);
    f32x4 acc[4][4];
#pragma unroll
    for (int i = 0; i < 4; ++i)
#pragma unroll
        for (int j = 0; j < 4; ++j) acc[i][j] = (f32x4){0.f, 0.f, 0.f, 0.f};
    g2_main(hbase, wbase, l, w < 3, acc);
    STAMP(3, 3);
#pragma unroll
    for (int i = 0; i < 4; ++i)
#pragma unroll
        for (int j = 0; j < 4; ++j) red[(w * 16 + i * 4 + j) * 64 + l] = acc[i][j];
    STAMP(3, 4);
    __syncthreads();
    STAMP(3, 5);
    vm_wait2<0>(xr0, xr1);
#pragma unroll
    for (int q = 0; q < 2; ++q) {
        const int tile = 2 * w + q;
        const u32x2 xr = q ? xr1 : xr0;
        f32x4 sum = red[tile * 64 + l];
#pragma unroll
        for (int p = 1; p < 8; ++p) sum += red[(p * 16 + tile) * 64 + l];
        u32x2 o;
        o[0] = pk2(bflo(xr[0]) + rbf(sum[0]), bfhi(xr[0]) + rbf(sum[1]));
        o[1] = pk2(bflo(xr[1]) + rbf(sum[2]), bfhi(xr[1]) + rbf(sum[3]));
        if (STATS) xout[(size_t)(n >> 2) * S_ + s0 + 16 * q] = o;
        else xout[((size_t)(s0 + 16 * q) * D_ + n) >> 2] = o;
        if (STATS) {
            const float v0 = bflo(o[0]), v1 = bfhi(o[0]), v2 = bflo(o[1]), v3 = bfhi(o[1]);
            const float sm = quadsum((v0 + v1) + (v2 + v3));
            const float sq = quadsum((v0 * v0 + v1 * v1) + (v2 * v2 + v3 * v3));
            if (l < 16) { f32x2 e = {sm, sq}; sred[(w >> 1) * 64 + 32 * (w & 1) + 16 * q + l] = e; }
        }
    }
    if (STATS) {
        __syncthreads();
        if (threadIdx.x < 64) {
            const f32x2 e = (sred[threadIdx.x] + sred[64 + threadIdx.x]) + (sred[128 + threadIdx.x] + sred[192 + threadIdx.x]);
            part[(size_t)(rb * 64 + threadIdx.x) * 8 + cb] = e;
        }
    }
    STAMP(3, 6);
}

extern "C" void kernel_launch(void* const* d_in, const int* in_sizes, int n_in,
                              void* d_out, int out_size, void* d_ws, size_t ws_size,
                              hipStream_t stream) {
    const u16* q       = (const u16*)d_in[0];
    const u16* k       = (const u16*)d_in[1];
    const u16* v       = (const u16*)d_in[2];
    const u16* shift_k = (const u16*)d_in[3];
    const u16* ssm_k   = (const u16*)d_in[4];
    const u16* ssm_D   = (const u16*)d_in[5];
    const u16* w_gate  = (const u16*)d_in[7];
    const u16* w_up    = (const u16*)d_in[8];
    const u16* w_down  = (const u16*)d_in[9];
    u16* out = (u16*)d_out;

    char* ws = (char*)d_ws;
    const size_t MB2 = (size_t)S_ * D_ * 2;
    u16*   kvR = (u16*)(ws);
    u16*   Y4  = (u16*)(ws + MB2);
    u16*   x1  = (u16*)(ws + 2 * MB2);
    u16*   x2  = (u16*)(ws + 3 * MB2);
    u16*   x3  = (u16*)(ws + 4 * MB2);
    u32x4* Wgu = (u32x4*)(ws + 5 * MB2);
    u32x4* Wd  = (u32x4*)(ws + 5 * MB2 + (size_t)NFT * KS1 * 2 * 1024);
    u32x4* hpk = (u32x4*)(ws + 5 * MB2 + (size_t)NFT * KS1 * 2 * 1024 + (size_t)32 * KS2 * 1024);

    char* tail = (char*)hpk + (size_t)128 * KS2 * 1024;
    float* csum  = (float*)tail;
    f32x2* part0 = (f32x2*)(tail + 16384);
    f32x2* part2 = (f32x2*)(tail + 16384 + 131072);

    u32x2* x0t = (u32x2*)(tail + 16384 + 2 * 131072);

    prep_kernel<<<256 + PACK_BLOCKS + 86 + 128, 256, 0, stream>>>(k, v, shift_k, w_gate, w_up, w_down, q, kvR, Wgu, Wd,
                                                                  csum, part0, x0t);
    conv_kernel<<<256, 256, 0, stream>>>(kvR, ssm_k, ssm_D, Y4);
    g1_kernel<<<256, 1024, 0, stream>>>(x0t, part0, (const u32x2*)Y4, Wgu, csum, (u32x2*)x1, (u32x2*)hpk);
    g2_kernel<<<256, 512, 0, stream>>>(hpk, Wd, (const u32x2*)x1, (u32x2*)x2, part2, 1);
    g1_kernel<<<256, 1024, 0, stream>>>((const u32x2*)x2, part2, (const u32x2*)Y4, Wgu, csum, (u32x2*)x3, (u32x2*)hpk);
    g2_kernel<<<256, 512, 0, stream>>>(hpk, Wd, (const u32x2*)x3, (u32x2*)out, part2, 0);
}
```

```cpp
#include <hip/hip_runtime.h>
#include <hip/hip_bf16.h>
#include <stdint.h>

typedef __attribute__((ext_vector_type(8))) __bf16   bf16x8;
typedef __attribute__((ext_vector_type(2))) __bf16   bf16x2;
typedef __attribute__((ext_vector_type(4))) float    f32x4;
typedef __attribute__((ext_vector_type(2))) float    f32x2;
typedef __attribute__((ext_vector_type(4))) unsigned u32x4;
typedef __attribute__((ext_vector_type(2))) unsigned u32x2;
typedef unsigned short u16;

constexpr int S_ = 2048, D_ = 512, F_ = 1376;
constexpr int NFT = 86;
constexpr int KS1 = 16;
constexpr int KS2 = 43;

__device__ __forceinline__ float bflo(unsigned u) { return __builtin_bit_cast(float, u << 16); }
__device__ __forceinline__ float bfhi(unsigned u) { return __builtin_bit_cast(float, u & 0xffff0000u); }
__device__ __forceinline__ unsigned pk2(float lo, float hi) {
    f32x2 v = {lo, hi};
    bf16x2 r = __builtin_convertvector(v, bf16x2);
    return __builtin_bit_cast(unsigned, r);
}
__device__ __forceinline__ float rbf(float x) { return bflo(pk2(x, x)); }
__device__ __forceinline__ f32x4 mfma16(u32x4 a, u32x4 b, f32x4 c) {
    return __builtin_amdgcn_mfma_f32_16x16x32_bf16(__builtin_bit_cast(bf16x8, a), __builtin_bit_cast(bf16x8, b), c, 0, 0, 0);
}

template <int IMM> __device__ __forceinline__ u32x4 gld16(const void* sbase, unsigned voff) {
    u32x4 r;
    asm volatile("global_load_dwordx4 %0, %1, %2 offset:%3" : "=v"(r) : "v"(voff), "s"(sbase), "i"(IMM) : "memory");
    return r;
}
template <int IMM> __device__ __forceinline__ u32x2 gld8(const void* sbase, unsigned voff) {
    u32x2 r;
    asm volatile("global_load_dwordx2 %0, %1, %2 offset:%3" : "=v"(r) : "v"(voff), "s"(sbase), "i"(IMM) : "memory");
    return r;
}
template <int N> __device__ __forceinline__ void vm_wait2(u32x4& a, u32x4& b) {
    asm volatile("s_waitcnt vmcnt(%2)" : "+v"(a), "+v"(b) : "i"(N) : "memory");
}
template <int N> __device__ __forceinline__ void vm_wait8(u32x4 (&a)[4], u32x4 (&b)[4]) {
    asm volatile("s_waitcnt vmcnt(%8)"
                 : "+v"(a[0]), "+v"(a[1]), "+v"(a[2]), "+v"(a[3]), "+v"(b[0]), "+v"(b[1]), "+v"(b[2]), "+v"(b[3])
                 : "i"(N) : "memory");
}
template <int N> __device__ __forceinline__ void vm_wait2(u32x2& a, u32x2& b) {
    asm volatile("s_waitcnt vmcnt(%2)" : "+v"(a), "+v"(b) : "i"(N) : "memory");
}

constexpr int PACK_GU_THREADS = 2 * 64 * 688;
constexpr int PACK_D_THREADS  = 172 * 256;
constexpr int PACK_BLOCKS = (PACK_GU_THREADS + PACK_D_THREADS) / 256;

__device__ __forceinline__ void pack_item(const u16* __restrict__ src, int N, int ko, int np, u32x4* __restrict__ dst0) {
    unsigned w[8];
#pragma unroll
    for (int j = 0; j < 8; ++j) w[j] = *reinterpret_cast<const unsigned*>(src + (size_t)(8 * ko + j) * N + 2 * np);
    u32x4 o0, o1;
#pragma unroll
    for (int c = 0; c < 4; ++c) {
        o0[c] = (w[2 * c] & 0xffffu) | (w[2 * c + 1] << 16);
        o1[c] = (w[2 * c] >> 16) | (w[2 * c + 1] & 0xffff0000u);
    }
    dst0[0] = o0;
    dst0[1] = o1;
}

__global__ __launch_bounds__(256) void prep_kernel(const u16* __restrict__ k, const u16* __restrict__ v,
                                                   const u16* __restrict__ shift_k,
                                                   const u16* __restrict__ wg, const u16* __restrict__ wu,
                                                   const u16* __restrict__ wd,
                                                   const u16* __restrict__ x0,
                                                   u16* __restrict__ kvR, u32x4* __restrict__ Wgu,
                                                   u32x4* __restrict__ Wd, float* __restrict__ csum,
                                                   f32x2* __restrict__ part0, u32x2* __restrict__ x0t) {
    __shared__ unsigned tile[64 * 33];
    const int t = threadIdx.x;
    if (blockIdx.x < 256) {
        const int st = blockIdx.x >> 3, dt = blockIdx.x & 7;
        const int s0 = st * 64, d0 = dt * 64;
        const int c8 = t & 7, r = t >> 3;
        const int sA = s0 + 2 * r;
        const int dd = d0 + 8 * c8;
        u32x4 kr[5], vr[2], wv[4];
#pragma unroll
        for (int i = 0; i < 5; ++i) {
            const int s = sA - 3 + i;
            u32x4 z = {0u, 0u, 0u, 0u};
            kr[i] = (s >= 0) ? *reinterpret_cast<const u32x4*>(k + (size_t)s * D_ + dd) : z;
        }
#pragma unroll
        for (int i = 0; i < 2; ++i) vr[i] = *reinterpret_cast<const u32x4*>(v + (size_t)(sA + i) * D_ + dd);
#pragma unroll
        for (int i = 0; i < 4; ++i) wv[i] = *reinterpret_cast<const u32x4*>(shift_k + (size_t)dd * 4 + 8 * i);
#pragma unroll
        for (int j = 0; j < 8; ++j) {
            const unsigned wa = wv[j >> 1][(j & 1) * 2], wb = wv[j >> 1][(j & 1) * 2 + 1];
            const float w0 = bflo(wa), w1 = bfhi(wa), w2 = bflo(wb), w3 = bfhi(wb);
            float kk[5];
#pragma unroll
            for (int i = 0; i < 5; ++i) kk[i] = (j & 1) ? bfhi(kr[i][j >> 1]) : bflo(kr[i][j >> 1]);
            float res[2];
#pragma unroll
            for (int q = 0; q < 2; ++q) {
                float acc = rbf(kk[q] * w3);
                acc = rbf(acc + rbf(kk[q + 1] * w2));
                acc = rbf(acc + rbf(kk[q + 2] * w1));
                acc = rbf(acc + rbf(kk[q + 3] * w0));
                const float vv = (j & 1) ? bfhi(vr[q][j >> 1]) : bflo(vr[q][j >> 1]);
                res[q] = acc * vv;
            }
            tile[(8 * c8 + j) * 33 + (31 - r)] = pk2(res[1], res[0]);
        }
        __syncthreads();
#pragma unroll
        for (int i = 0; i < 2; ++i) {
            const int idx = t + 256 * i;
            const int d = idx >> 3, q = idx & 7;
            u32x4 o;
#pragma unroll
            for (int c = 0; c < 4; ++c) o[c] = tile[d * 33 + 4 * q + c];
            *reinterpret_cast<u32x4*>(kvR + (size_t)(d0 + d) * S_ + (S_ - 64 - s0) + 8 * q) = o;
        }
    } else if (blockIdx.x >= 256 + PACK_BLOCKS + 86) {
        const int r0 = (blockIdx.x - (256 + PACK_BLOCKS + 86)) * 16;
        const int row = t & 15;
        f32x2* sp = reinterpret_cast<f32x2*>(tile);
        u32x4 a[4];
#pragma unroll
        for (int i = 0; i < 4; ++i) a[i] = *reinterpret_cast<const u32x4*>(x0 + (size_t)(r0 + row) * D_ + 8 * ((t >> 4) + 16 * i));
#pragma unroll
        for (int i = 0; i < 4; ++i) {
            const int o = (t >> 4) + 16 * i;
            u32x2 lo = {a[i][0], a[i][1]}, hi = {a[i][2], a[i][3]};
            x0t[(size_t)(2 * o) * S_ + r0 + row] = lo;
            x0t[(size_t)(2 * o + 1) * S_ + r0 + row] = hi;
            float sm = 0.f, sq = 0.f;
#pragma unroll
            for (int j = 0; j < 4; ++j) {
                const float l0 = bflo(a[i][j]), h0 = bfhi(a[i][j]);
                sm += l0 + h0;
                sq += l0 * l0 + h0 * h0;
            }
            f32x2 e = {sm, sq};
            sp[row * 64 + o] = e;
        }
        __syncthreads();
        if (t < 128) {
            const int rr = t >> 3, g = t & 7;
            f32x2 e = sp[rr * 64 + 8 * g];
#pragma unroll
            for (int j = 1; j < 8; ++j) e += sp[rr * 64 + 8 * g + j];
            part0[(size_t)(r0 + rr) * 8 + g] = e;
        }
    } else if (blockIdx.x >= 256 + PACK_BLOCKS) {
        const int cbk = blockIdx.x - (256 + PACK_BLOCKS);
        const int m = cbk / 43, f0 = (cbk % 43) * 32;
        const u16* src = m ? wu : wg;
        const int np = t & 15, kg = t >> 4;
        unsigned wv[32];
#pragma unroll
        for (int j = 0; j < 32; ++j) wv[j] = *reinterpret_cast<const unsigned*>(src + (size_t)(32 * kg + j) * F_ + f0 + 2 * np);
        float s0 = 0.f, s1 = 0.f;
#pragma unroll
        for (int j = 0; j < 32; ++j) { s0 += bflo(wv[j]); s1 += bfhi(wv[j]); }
        float* red = reinterpret_cast<float*>(tile);
        red[kg * 33 + 2 * np] = s0;
        red[kg * 33 + 2 * np + 1] = s1;
        __syncthreads();
        if (t < 32) {
            float a = 0.f;
#pragma unroll
            for (int j = 0; j < 16; ++j) a += red[j * 33 + t];
            csum[m * F_ + f0 + t] = a;
        }
    } else {
        int id = (blockIdx.x - 256) * 256 + t;
        if (id < PACK_GU_THREADS) {
            const int np = id % 688;
            const int rest = id / 688;
            const int ko = rest & 63, m = rest >> 6;
            const int n = 2 * np;
            const int ft = n >> 4, ks = ko >> 2, g = ko & 3;
            pack_item(m ? wu : wg, F_, ko, np, Wgu + ((size_t)((ft * KS1 + ks) * 2 + m) * 64 + g * 16 + (n & 15)));
        } else {
            id -= PACK_GU_THREADS;
            const int np = id & 255;
            const int ko = id >> 8;
            const int n = 2 * np;
            const int nt = n >> 4, ks = ko >> 2, g = ko & 3;
            pack_item(wd, D_, ko, np, Wd + ((size_t)(nt * KS2 + ks) * 64 + g * 16 + (n & 15)));
        }
    }
}

constexpr int CV_C = 4160;
constexpr int CV_U = 4 * CV_C;
constexpr int CV_CH = CV_U + 4608;
constexpr int CV_Y = 4 * CV_CH;

template <int OFF> __device__ __forceinline__ u32x2 lds8(unsigned addr) {
    u32x2 r;
    asm volatile("ds_read_b64 %0, %1 offset:%2" : "=v"(r) : "v"(addr), "i"(OFF) : "memory");
    return r;
}
template <int OFF> __device__ __forceinline__ u32x4 lds16(unsigned addr) {
    u32x4 r;
    asm volatile("ds_read_b128 %0, %1 offset:%2" : "=v"(r) : "v"(addr), "i"(OFF) : "memory");
    return r;
}
__device__ __forceinline__ void lgkm_wait(u32x2 (&a)[16], u32x4 (&b)[8]) {
    asm volatile("s_waitcnt lgkmcnt(0)"
                 : "+v"(a[0]), "+v"(a[1]), "+v"(a[2]), "+v"(a[3]), "+v"(a[4]), "+v"(a[5]), "+v"(a[6]), "+v"(a[7]),
                   "+v"(a[8]), "+v"(a[9]), "+v"(a[10]), "+v"(a[11]), "+v"(a[12]), "+v"(a[13]), "+v"(a[14]), "+v"(a[15]),
                   "+v"(b[0]), "+v"(b[1]), "+v"(b[2]), "+v"(b[3]), "+v"(b[4]), "+v"(b[5]), "+v"(b[6]), "+v"(b[7])
                 :: "memory");
}
template <int BLK, int GBLK>
__device__ __forceinline__ void conv_load(unsigned fa, unsigned ga, u32x2 (&F)[16], u32x4 (&G)[8]) {
#define CVL(i) F[2 * (i)] = lds8<64 * (8 * BLK + (i))>(fa); F[2 * (i) + 1] = lds8<64 * (8 * BLK + (i)) + 8>(fa); \
               G[i] = lds16<64 * (8 * (GBLK + 7) + (i))>(ga);
    CVL(0) CVL(1) CVL(2) CVL(3) CVL(4) CVL(5) CVL(6) CVL(7)
#undef CVL
}
__device__ __forceinline__ u32x4 cat(u32x2 a, u32x2 b) { u32x4 r = {a[0], a[1], b[0], b[1]}; return r; }

template <int N, int T>
__device__ __forceinline__ void conv_step(unsigned fa, unsigned ga, u32x2 (&Fr)[3][16], u32x4 (&Gr)[2][8], f32x4& acc1,
                                          f32x4& acc0) {
    if (T >= 0) lgkm_wait(Fr[T % 3], Gr[T & 1]);
    if (T + 1 <= N + 1) conv_load<T + 1, T - N>(fa, ga, Fr[(T + 1) % 3], Gr[(T + 1) & 1]);
    __builtin_amdgcn_sched_barrier(0);
    if (T >= 0) {
#pragma unroll
        for (int i = 0; i < 8; ++i) {
            acc1 = mfma16(cat(Fr[T % 3][2 * i], Fr[T % 3][2 * i + 1]), Gr[T & 1][i], acc1);
            if (T >= 1) acc0 = mfma16(cat(Fr[(T + 2) % 3][2 * i], Fr[(T + 2) % 3][2 * i + 1]), Gr[T & 1][i], acc0);
        }
    }
    __builtin_amdgcn_sched_barrier(0);
    if constexpr (T < N + 1) conv_step<N, T + 1>(fa, ga, Fr, Gr, acc1, acc0);
}
template <int N>
__device__ __forceinline__ void conv_task(unsigned fa, unsigned ga, f32x4& acc1, f32x4& acc0) {
    u32x2 Fr[3][16];
    u32x4 Gr[2][8];
    conv_step<N, -1>(fa, ga, Fr, Gr, acc1, acc0);
}

__device__ __forceinline__ unsigned sh16(unsigned lo, unsigned hi) { return (lo >> 16) | (hi << 16); }

__global__ __launch_bounds__(256) void conv_kernel(const u16* __restrict__ kvR, const u16* __restrict__ ssm_k,
                                                   const u16* __restrict__ ssm_D, u16* __restrict__ Y4) {
    __shared__ __attribute__((aligned(16))) char smem[CV_Y + 8704];
    const int t = threadIdx.x, l = t & 63;
    const int w = __builtin_amdgcn_readfirstlane(t >> 6);
    const int cq = blockIdx.x >> 1, half = blockIdx.x & 1;
    const int ch = 4 * cq + w;
    char* base = smem + w * CV_CH;
    const float Dv = bflo((unsigned)ssm_D[ch]);
    {
        const u16* hp = ssm_k + (size_t)ch * S_;
        const u16* up = kvR + (size_t)ch * S_;
        u32x4 hv[4], uv[4];
        u32x2 hx[4];
#pragma unroll
        for (int i = 0; i < 4; ++i) {
            const int m = l + 64 * i;
            hv[i] = *reinterpret_cast<const u32x4*>(hp + 8 * m);
            uv[i] = *reinterpret_cast<const u32x4*>(up + 8 * m);
            const u32x2 zz = {0u, 0u};
            hx[i] = (m < 255) ? *reinterpret_cast<const u32x2*>(hp + 8 * m + 8) : zz;
        }
        const u32x4 z = {0u, 0u, 0u, 0u};
        if (l < 32) *reinterpret_cast<u32x4*>(base + CV_U + 4096 + 16 * l) = z;
        if (l < 4) *reinterpret_cast<u32x4*>(base + l * CV_C) = z;
        if (l == 0) {
            const unsigned h01 = hv[0][0], h23 = hv[0][1];
            u32x4 c0 = z, c1 = z, c2 = z, c3 = z;
            c0[3] = h01 << 16;
            c1[3] = h01;
            c2[2] = h01 << 16; c2[3] = sh16(h01, h23);
            c3[2] = h01;       c3[3] = h23;
            *reinterpret_cast<u32x4*>(base + 0 * CV_C + 16) = c0;
            *reinterpret_cast<u32x4*>(base + 1 * CV_C + 16) = c1;
            *reinterpret_cast<u32x4*>(base + 2 * CV_C + 16) = c2;
            *reinterpret_cast<u32x4*>(base + 3 * CV_C + 16) = c3;
        }
#pragma unroll
        for (int i = 0; i < 4; ++i) {
            const int m = l + 64 * i;
            const unsigned d0 = hv[i][0], d1 = hv[i][1], d2 = hv[i][2], d3 = hv[i][3], d4 = hx[i][0], d5 = hx[i][1];
            const unsigned e0 = sh16(d0, d1), e1 = sh16(d1, d2), e2 = sh16(d2, d3), e3 = sh16(d3, d4), e4 = sh16(d4, d5);
            u32x4 c0 = {e0, e1, e2, e3}, c1 = {d1, d2, d3, d4}, c2 = {e1, e2, e3, e4}, c3 = {d2, d3, d4, d5};
            *reinterpret_cast<u32x4*>(base + 0 * CV_C + 32 + 16 * m) = c0;
            *reinterpret_cast<u32x4*>(base + 1 * CV_C + 32 + 16 * m) = c1;
            *reinterpret_cast<u32x4*>(base + 2 * CV_C + 32 + 16 * m) = c2;
            *reinterpret_cast<u32x4*>(base + 3 * CV_C + 32 + 16 * m) = c3;
            *reinterpret_cast<u32x4*>(base + CV_U + 16 * m) = uv[i];
        }
    }
    __syncthreads();

    const int o0 = (l & 15) + 8 * (l >> 4);
    const unsigned fp = (unsigned)(size_t)(base + (l & 3) * CV_C + 2 * (o0 & ~3));
    const unsigned gp = (unsigned)(size_t)(base + CV_U + 2 * (2032 - 16 * (l & 15) + 8 * (l >> 4)) - 64 * 56);
    f32x4 a0 = {0.f, 0.f, 0.f, 0.f}, a1 = a0, a2 = a0, a3 = a0;
    int n0, n1, n2, n3;
    if (half == 0) {
        n0 = 7; n1 = 6; n2 = 1; n3 = 0;
        conv_task<6>(fp, gp, a0, a1);
        conv_task<0>(fp, gp, a2, a3);
    } else {
        n0 = 5; n1 = 4; n2 = 3; n3 = 2;
        conv_task<4>(fp, gp, a0, a1);
        conv_task<2>(fp, gp, a2, a3);
    }
    u16* yb = reinterpret_cast<u16*>(smem + CV_Y);
    const int sl = 16 * (l & 15) + 4 * (l >> 4);
    const int slp = sl + (l & 15);
#define EPI(acc, n, i)                                                                        \
    {                                                                                         \
        const int s = 256 * (n) + sl;                                                         \
        const u32x2 uu = *reinterpret_cast<const u32x2*>(base + CV_U + 2 * (2044 - s));       \
        const float u0 = bfhi(uu[1]), u1 = bflo(uu[1]), u2 = bfhi(uu[0]), u3 = bflo(uu[0]);   \
        const unsigned c01 = pk2(acc[0], acc[1]), c23 = pk2(acc[2], acc[3]);                  \
        const unsigned k01 = pk2(u0 * Dv, u1 * Dv), k23 = pk2(u2 * Dv, u3 * Dv);              \
        const unsigned p01 = pk2(bflo(c01) + bflo(k01), bfhi(c01) + bfhi(k01));               \
        const unsigned p23 = pk2(bflo(c23) + bflo(k23), bfhi(c23) + bfhi(k23));               \
        u16* q = yb + ((i) * 272 + slp) * 4 + w;                                              \
        q[0] = (u16)p01; q[4] = (u16)(p01 >> 16); q[8] = (u16)p23; q[12] = (u16)(p23 >> 16);  \
    }
    EPI(a0, n0, 0) EPI(a1, n1, 1) EPI(a2, n2, 2) EPI(a3, n3, 3)
    __syncthreads();
    {
        const u32x2* yr = reinterpret_cast<const u32x2*>(smem + CV_Y) + t + (t >> 4);
        u32x2* dst = reinterpret_cast<u32x2*>(Y4) + (size_t)cq * S_;
        dst[256 * n0 + t] = yr[0];
        dst[256 * n1 + t] = yr[272];
        dst[256 * n2 + t] = yr[544];
        dst[256 * n3 + t] = yr[816];
    }
}

__device__ __forceinline__ float rowsum8(float v) {
    v += __builtin_bit_cast(float, __builtin_amdgcn_update_dpp(0, __builtin_bit_cast(int, v), 0x128, 0xf, 0xf, false));
    {
        const unsigned u = __builtin_bit_cast(unsigned, v);
        auto r = __builtin_amdgcn_permlane16_swap(u, u, false, false);
        v = __builtin_bit_cast(float, (unsigned)r[0]) + __builtin_bit_cast(float, (unsigned)r[1]);
    }
    {
        const unsigned u = __builtin_bit_cast(unsigned, v);
        auto r = __builtin_amdgcn_permlane32_swap(u, u, false, false);
        v = __builtin_bit_cast(float, (unsigned)r[0]) + __builtin_bit_cast(float, (unsigned)r[1]);
    }
    return v;
}
__device__ __forceinline__ f32x2 unpk(unsigned u) { f32x2 r = {bflo(u), bfhi(u)}; return r; }

__device__ __forceinline__ void g1_epilogue(const f32x4 (&ag)[4], const f32x4 (&au)[4], int rb, int ft, int l,
                                            u32x2* __restrict__ hpk, const f32x2* st2, u32x4 cgu, u32x4 cuu) {
    const int ks2 = ft >> 1;
    const int slot = (2 * (ft & 1) + (l >> 5)) * 16 + (l & 15);
    const f32x4 cg = __builtin_bit_cast(f32x4, cgu), cu = __builtin_bit_cast(f32x4, cuu);
    float sa[4], sb[4];
#pragma unroll
    for (int rt = 0; rt < 4; ++rt) {
        const f32x4 pp = *reinterpret_cast<const f32x4*>(st2 + 2 * (16 * rt + (l & 15)));
        const float mean2 = (pp[0] + pp[2]) * (1.0f / 512.0f);
        const float inv2 = __builtin_amdgcn_rsqf(fmaxf((pp[1] + pp[3]) * (1.0f / 512.0f) - mean2 * mean2, 0.f) + 1e-5f);
        sa[rt] = inv2;
        sb[rt] = -mean2 * inv2;
    }
    u32x2* dst = hpk + ((size_t)(rb * 4 * KS2 + ks2) * 64 + slot) * 2 + ((l >> 4) & 1);
#pragma unroll
    for (int rt = 0; rt < 4; ++rt) {
        const f32x4 g4 = ag[rt] * sa[rt] + cg * sb[rt];
        const f32x4 u4 = au[rt] * sa[rt] + cu * sb[rt];
        const unsigned g01 = pk2(g4[0], g4[1]), g23 = pk2(g4[2], g4[3]);
        const unsigned u01 = pk2(u4[0], u4[1]), u23 = pk2(u4[2], u4[3]);
        const float g0 = bflo(g01), g1 = bfhi(g01), g2 = bflo(g23), g3 = bfhi(g23);
        const float e0 = __expf(-g0), e1 = __expf(-g1), e2 = __expf(-g2), e3 = __expf(-g3);
        const float r0 = __builtin_amdgcn_rcpf(1.0f + e0), r1 = __builtin_amdgcn_rcpf(1.0f + e1);
        const float r2 = __builtin_amdgcn_rcpf(1.0f + e2), r3 = __builtin_amdgcn_rcpf(1.0f + e3);
        const unsigned s01 = pk2(g0 * r0, g1 * r1), s23 = pk2(g2 * r2, g3 * r3);
        u32x2 o;
        o[0] = pk2(bflo(s01) * bflo(u01), bfhi(s01) * bfhi(u01));
        o[1] = pk2(bflo(s23) * bflo(u23), bfhi(s23) * bfhi(u23));
        dst[(size_t)rt * KS2 * 128] = o;
    }
}

constexpr int G1_WIN = 6;
__device__ constexpr int g1_ks(int t) { return t < 4 ? t : t < 8 ? t + 4 : t < 12 ? t - 4 : t; }
__device__ constexpr int g1_younger(int t) { return 2 * ((t - 1 + G1_WIN < 15 ? t - 1 + G1_WIN : 15) - t); }
#define G1_LOADW(t)                                                                                           \
    {                                                                                                         \
        wf[(t) % G1_WIN][0] = gld16<((2 * g1_ks(t)) & 3) * 1024>(wbase, wo + ((2 * g1_ks(t)) >> 2) * 4096);     \
        wf[(t) % G1_WIN][1] = gld16<((2 * g1_ks(t) + 1) & 3) * 1024>(wbase, wo + ((2 * g1_ks(t)) >> 2) * 4096); \
    }
#define G1_STEP(t)                                                                            \
    {                                                                                         \
        vm_wait2<g1_younger(t)>(wf[(t) % G1_WIN][0], wf[(t) % G1_WIN][1]);                    \
        _Pragma("unroll") for (int rt = 0; rt < 4; ++rt) {                                    \
            const u32x4 bb = At[(rt * KS1 + g1_ks(t)) * 64 + l];                              \
            ag[rt] = mfma16(wf[(t) % G1_WIN][0], bb, ag[rt]);                                 \
            au[rt] = mfma16(wf[(t) % G1_WIN][1], bb, au[rt]);                                 \
        }                                                                                     \
        if ((t) + G1_WIN < 16) G1_LOADW((t) + G1_WIN)                                         \
    }
#define G1_STEPS8(b)                                                                          \
    G1_STEP(b + 0) G1_STEP(b + 1) G1_STEP(b + 2) G1_STEP(b + 3) G1_STEP(b + 4) G1_STEP(b + 5) \
    G1_STEP(b + 6) G1_STEP(b + 7)

template <bool LATE>
__device__ __forceinline__ void g1_mfma_role(const u32x4* At, const f32x2* st2, const u32x4* wbase,
                                             const float* csum, int l, int rb, int ft, u32x2* __restrict__ hpk) {
    const unsigned wo = 16 * l;
    u32x4 wf[G1_WIN][2];
    const unsigned co = (16 * ft + 4 * (l >> 4)) * 4;
    u32x4 cs0 = gld16<0>(csum, co);
    u32x4 cs1 = gld16<0>(csum, co + F_ * 4);
    G1_LOADW(0) G1_LOADW(1) G1_LOADW(2) G1_LOADW(3)
    __syncthreads();
    G1_LOADW(4) G1_LOADW(5)
    f32x4 ag[4], au[4];
#pragma unroll
    for (int r = 0; r < 4; ++r) { ag[r] = (f32x4){0.f, 0.f, 0.f, 0.f}; au[r] = ag[r]; }
    G1_STEPS8(0)
    if (!LATE) __syncthreads();
    G1_STEPS8(8)
    vm_wait2<0>(cs0, cs1);
    g1_epilogue(ag, au, rb, ft, l, hpk, st2, cs0, cs1);
}

__global__ __launch_bounds__(1024, 4) void g1_kernel(const u32x2* __restrict__ x, const f32x2* __restrict__ part,
                                                     const u32x2* __restrict__ Y4, const u32x4* __restrict__ Wgu,
                                                     const float* __restrict__ csum, u32x2* __restrict__ x1out,
                                                     u32x2* __restrict__ hpk) {
    __shared__ u32x4 At[4 * KS1 * 64 + 64];
    f32x2* st2 = reinterpret_cast<f32x2*>(At + 4 * KS1 * 64);
    const int b = blockIdx.x;
    const int rb = (b & 7) * 4 + (b >> 6), cb = (b >> 3) & 7;
    const int l = threadIdx.x & 63;
    const int w = __builtin_amdgcn_readfirstlane(threadIdx.x >> 6);
    const int ntl = cb < 6 ? 11 : 10;
    const int ft0 = cb < 6 ? 11 * cb : 66 + 10 * (cb - 6);
    if (w < 8) {
        const int ip = l & 7, g = l >> 3;
        const int rt = w >> 1, dh = w & 1;
        const int row = rb * 64 + 16 * rt + 2 * ip;
        u32x4 xv[8], yv[8];
        u32x2 pa = gld8<0>(part, (unsigned)(row * 8 + g) * 8);
        u32x2 pb = gld8<64>(part, (unsigned)(row * 8 + g) * 8);
        const unsigned o0 = (unsigned)((64 * dh + g) * S_ + row) * 8;
#pragma unroll
        for (int c = 0; c < 8; ++c) {
            xv[c] = gld16<0>(x, o0 + c * (8 * S_ * 8));
            yv[c] = gld16<0>(Y4, o0 + c * (8 * S_ * 8));
        }
        vm_wait2<16>(pa, pb);
        const float meanA = rowsum8(__builtin_bit_cast(float, (unsigned)pa[0])) * (1.0f / 512.0f);
        const float msqA = rowsum8(__builtin_bit_cast(float, (unsigned)pa[1])) * (1.0f / 512.0f);
        const float invA = 1.0f / sqrtf(fmaxf(msqA - meanA * meanA, 0.f) + 1e-5f);
        const float meanB = rowsum8(__builtin_bit_cast(float, (unsigned)pb[0])) * (1.0f / 512.0f);
        const float msqB = rowsum8(__builtin_bit_cast(float, (unsigned)pb[1])) * (1.0f / 512.0f);
        const float invB = 1.0f / sqrtf(fmaxf(msqB - meanB * meanB, 0.f) + 1e-5f);
        const f32x2 iA = {invA, invA}, bA = {-meanA * invA, -meanA * invA};
        const f32x2 iB = {invB, invB}, bB = {-meanB * invB, -meanB * invB};
        f32x2 sa = {0.f, 0.f}, sb = sa, qa = sa, qb = sa;
        u32x2* At2 = reinterpret_cast<u32x2*>(At);
#define G1_TRANS(c)                                                                                   \
        {                                                                                             \
            vm_wait2<14 - 2 * (c)>(xv[c], yv[c]);                                                     \
            u32x4 o;                                                                                  \
            _Pragma("unroll") for (int j = 0; j < 4; ++j) {                                           \
                const f32x2 xx = unpk(xv[c][j]);                                                      \
                const f32x2 yy = unpk(yv[c][j]);                                                      \
                const f32x2 q = (j < 2) ? (xx * iA + bA) : (xx * iB + bB);                            \
                const f32x2 r = xx + q * yy;                                                          \
                o[j] = pk2(r[0], r[1]);                                                               \
                if (j < 2) { sa += r; qa += r * r; } else { sb += r; qb += r * r; }                   \
            }                                                                                         \
            const int unit = (rt * KS1 + 8 * dh + (c)) * 64 + (g >> 1) * 16 + 2 * ip;                 \
            u32x2 oa = {o[0], o[1]}, ob = {o[2], o[3]};                                               \
            At2[2 * unit + (g & 1)] = oa;                                                             \
            At2[2 * (unit + 1) + (g & 1)] = ob;                                                       \
            xv[c] = o;                                                                                \
        }
        G1_TRANS(0) G1_TRANS(1) G1_TRANS(2) G1_TRANS(3)
        __syncthreads();
        G1_TRANS(4) G1_TRANS(5) G1_TRANS(6) G1_TRANS(7)
#pragma unroll
        for (int c = 0; c < 8; ++c)
            if ((8 * dh + c) >> 1 == cb)
                *reinterpret_cast<u32x4*>(x1out + (size_t)(64 * dh + 8 * c + g) * S_ + row) = xv[c];
        const float s_a = rowsum8(sa[0] + sa[1]), q_a = rowsum8(qa[0] + qa[1]);
        const float s_b = rowsum8(sb[0] + sb[1]), q_b = rowsum8(qb[0] + qb[1]);
        if (g == 0) {
            f32x2 ea = {s_a, q_a}, eb = {s_b, q_b};
            st2[2 * (16 * rt + 2 * ip) + dh] = ea;
            st2[2 * (16 * rt + 2 * ip + 1) + dh] = eb;
        }
        if (w + 8 < ntl) g1_mfma_role<true>(At, st2, Wgu + (size_t)(ft0 + 8 + w) * (KS1 * 2 * 64), csum, l, rb, ft0 + 8 + w, hpk);
        else __syncthreads();
    } else {
        g1_mfma_role<false>(At, st2, Wgu + (size_t)(ft0 + w - 8) * (KS1 * 2 * 64), csum, l, rb, ft0 + w - 8, hpk);
    }
}

__device__ __forceinline__ void g2_main(const u32x4* hbase, const u32x4* wbase, int l, bool six, f32x4 (&acc)[4][4]) {
    u32x4 af[5][4], bf[5][4];
    unsigned vo[4];
#pragma unroll
    for (int i = 0; i < 4; ++i) vo[i] = 16 * l + i * (KS2 * 1024);
#define G2_LOAD(st)                                                                                   \
    _Pragma("unroll") for (int i = 0; i < 4; ++i) {                                                   \
        af[(st) % 5][i] = gld16<((st) & 3) * 1024>(wbase, vo[i] + ((st) >> 2) * 4096);                \
        bf[(st) % 5][i] = gld16<((st) & 3) * 1024>(hbase, vo[i] + ((st) >> 2) * 4096);                \
    }
#define G2_STEP(st, nwait)                                                                            \
    {                                                                                                 \
        vm_wait8<nwait>(af[(st) % 5], bf[(st) % 5]);                                                  \
        _Pragma("unroll") for (int i = 0; i < 4; ++i)                                                 \
            _Pragma("unroll") for (int j = 0; j < 4; ++j)                                             \
                acc[i][j] = mfma16(af[(st) % 5][i], bf[(st) % 5][j], acc[i][j]);                      \
        __builtin_amdgcn_sched_barrier(0);                                                            \
    }
    G2_LOAD(0) G2_LOAD(1) G2_LOAD(2) G2_LOAD(3) G2_LOAD(4)
    G2_STEP(0, 32)
    if (six) { G2_LOAD(5) }
    G2_STEP(1, 24) G2_STEP(2, 16) G2_STEP(3, 8) G2_STEP(4, 0)
    if (six) { G2_STEP(5, 0) }
}

__device__ __forceinline__ float quadsum(float v) {
    {
        const unsigned u = __builtin_bit_cast(unsigned, v);
        auto r = __builtin_amdgcn_permlane16_swap(u, u, false, false);
        v = __builtin_bit_cast(float, (unsigned)r[0]) + __builtin_bit_cast(float, (unsigned)r[1]);
    }
    {
        const unsigned u = __builtin_bit_cast(unsigned, v);
        auto r = __builtin_amdgcn_permlane32_swap(u, u, false, false);
        v = __builtin_bit_cast(float, (unsigned)r[0]) + __builtin_bit_cast(float, (unsigned)r[1]);
    }
    return v;
}

__global__ __launch_bounds__(512, 2) void g2_kernel(const u32x4* __restrict__ hpk, const u32x4* __restrict__ Wd,
                                                    const u32x2* __restrict__ xin, u32x2* __restrict__ xout,
                                                    f32x2* __restrict__ part, int STATS) {
    __shared__ f32x4 red[8 * 16 * 64 + 128];
    f32x2* sred = reinterpret_cast<f32x2*>(red + 8 * 16 * 64);
    const int b = blockIdx.x;
    const int rb = (b & 7) * 4 + (b >> 6), cb = (b >> 3) & 7;
    const int l = threadIdx.x & 63;
    const int w = __builtin_amdgcn_readfirstlane(threadIdx.x >> 6);
    const int start = 5 * w + (w < 3 ? w : 3);
    const u32x4* hbase = hpk + ((size_t)(rb * 4) * KS2 + start) * 64;
    const u32x4* wbase = Wd + ((size_t)(cb * 4) * KS2 + start) * 64;
    const int n = cb * 64 + 16 * (w >> 1) + 4 * (l >> 4);
    const int s0 = rb * 64 + 32 * (w & 1) + (l & 15);
    const unsigned xo = (unsigned)((n >> 2) * S_ + s0) * 8;
    u32x2 xr0 = gld8<0>(xin, xo), xr1 = gld8<128>(xin, xo);
    f32x4 acc[4][4];
#pragma unroll
    for (int i = 0; i < 4; ++i)
#pragma unroll
        for (int j = 0; j < 4; ++j) acc[i][j] = (f32x4){0.f, 0.f, 0.f, 0.f};
    g2_main(hbase, wbase, l, w < 3, acc);
#pragma unroll
    for (int i = 0; i < 4; ++i)
#pragma unroll
        for (int j = 0; j < 4; ++j) red[(w * 16 + i * 4 + j) * 64 + l] = acc[i][j];
    __syncthreads();
    vm_wait2<0>(xr0, xr1);
#pragma unroll
    for (int q = 0; q < 2; ++q) {
        const int tile = 2 * w + q;
        const u32x2 xr = q ? xr1 : xr0;
        f32x4 sum = red[tile * 64 + l];
#pragma unroll
        for (int p = 1; p < 8; ++p) sum += red[(p * 16 + tile) * 64 + l];
        u32x2 o;
        o[0] = pk2(bflo(xr[0]) + rbf(sum[0]), bfhi(xr[0]) + rbf(sum[1]));
        o[1] = pk2(bflo(xr[1]) + rbf(sum[2]), bfhi(xr[1]) + rbf(sum[3]));
        if (STATS) xout[(size_t)(n >> 2) * S_ + s0 + 16 * q] = o;
        else xout[((size_t)(s0 + 16 * q) * D_ + n) >> 2] = o;
        if (STATS) {
            const float v0 = bflo(o[0]), v1 = bfhi(o[0]), v2 = bflo(o[1]), v3 = bfhi(o[1]);
            const float sm = quadsum((v0 + v1) + (v2 + v3));
            const float sq = quadsum((v0 * v0 + v1 * v1) + (v2 * v2 + v3 * v3));
            if (l < 16) { f32x2 e = {sm, sq}; sred[(w >> 1) * 64 + 32 * (w & 1) + 16 * q + l] = e; }
        }
    }
    if (STATS) {
        __syncthreads();
        if (threadIdx.x < 64) {
            const f32x2 e = (sred[threadIdx.x] + sred[64 + threadIdx.x]) + (sred[128 + threadIdx.x] + sred[192 + threadIdx.x]);
            part[(size_t)(rb * 64 + threadIdx.x) * 8 + cb] = e;
        }
    }
}

extern "C" void kernel_launch(void* const* d_in, const int* in_sizes, int n_in,
                              void* d_out, int out_size, void* d_ws, size_t ws_size,
                              hipStream_t stream) {
    const u16* q       = (const u16*)d_in[0];
    const u16* k       = (const u16*)d_in[1];
    const u16* v       = (const u16*)d_in[2];
    const u16* shift_k = (const u16*)d_in[3];
    const u16* ssm_k   = (const u16*)d_in[4];
    const u16* ssm_D   = (const u16*)d_in[5];
    const u16* w_gate  = (const u16*)d_in[7];
    const u16* w_up    = (const u16*)d_in[8];
    const u16* w_down  = (const u16*)d_in[9];
    u16* out = (u16*)d_out;

    char* ws = (char*)d_ws;
    const size_t MB2 = (size_t)S_ * D_ * 2;
    u16*   kvR = (u16*)(ws);
    u16*   Y4  = (u16*)(ws + MB2);
    u16*   x1  = (u16*)(ws + 2 * MB2);
    u16*   x2  = (u16*)(ws + 3 * MB2);
    u16*   x3  = (u16*)(ws + 4 * MB2);
    u32x4* Wgu = (u32x4*)(ws + 5 * MB2);
    u32x4* Wd  = (u32x4*)(ws + 5 * MB2 + (size_t)NFT * KS1 * 2 * 1024);
    u32x4* hpk = (u32x4*)(ws + 5 * MB2 + (size_t)NFT * KS1 * 2 * 1024 + (size_t)32 * KS2 * 1024);

    char* tail = (char*)hpk + (size_t)128 * KS2 * 1024;
    float* csum  = (float*)tail;
    f32x2* part0 = (f32x2*)(tail + 16384);
    f32x2* part2 = (f32x2*)(tail + 16384 + 131072);

    u32x2* x0t = (u32x2*)(tail + 16384 + 2 * 131072);

    prep_kernel<<<256 + PACK_BLOCKS + 86 + 128, 256, 0, stream>>>(k, v, shift_k, w_gate, w_up, w_down, q, kvR, Wgu, Wd,
                                                                  csum, part0, x0t);
    conv_kernel<<<256, 256, 0, stream>>>(kvR, ssm_k, ssm_D, Y4);
    g1_kernel<<<256, 1024, 0, stream>>>(x0t, part0, (const u32x2*)Y4, Wgu, csum, (u32x2*)x1, (u32x2*)hpk);
    g2_kernel<<<256, 512, 0, stream>>>(hpk, Wd, (const u32x2*)x1, (u32x2*)x2, part2, 1);
    g1_kernel<<<256, 1024, 0, stream>>>((const u32x2*)x2, part2, (const u32x2*)Y4, Wgu, csum, (u32x2*)x3, (u32x2*)hpk);
    g2_kernel<<<256, 512, 0, stream>>>(hpk, Wd, (const u32x2*)x3, (u32x2*)out, part2, 0);
}
```

```cpp
#include <hip/hip_runtime.h>
#include <hip/hip_bf16.h>
#include <stdint.h>

typedef __attribute__((ext_vector_type(8))) __bf16   bf16x8;
typedef __attribute__((ext_vector_type(2))) __bf16   bf16x2;
typedef __attribute__((ext_vector_type(4))) float    f32x4;
typedef __attribute__((ext_vector_type(2))) float    f32x2;
typedef __attribute__((ext_vector_type(4))) unsigned u32x4;
typedef __attribute__((ext_vector_type(2))) unsigned u32x2;
typedef unsigned short u16;

constexpr int S_ = 2048, D_ = 512, F_ = 1376;
constexpr int NFT = 86;
constexpr int KS1 = 16;
constexpr int KS2 = 43;

__device__ __forceinline__ float bflo(unsigned u) { return __builtin_bit_cast(float, u << 16); }
__device__ __forceinline__ float bfhi(unsigned u) { return __builtin_bit_cast(float, u & 0xffff0000u); }
__device__ __forceinline__ unsigned pk2(float lo, float hi) {
    f32x2 v = {lo, hi};
    bf16x2 r = __builtin_convertvector(v, bf16x2);
    return __builtin_bit_cast(unsigned, r);
}
__device__ __forceinline__ float rbf(float x) { return bflo(pk2(x, x)); }
__device__ __forceinline__ f32x4 mfma16(u32x4 a, u32x4 b, f32x4 c) {
    return __builtin_amdgcn_mfma_f32_16x16x32_bf16(__builtin_bit_cast(bf16x8, a), __builtin_bit_cast(bf16x8, b), c, 0, 0, 0);
}

template <int IMM> __device__ __forceinline__ u32x4 gld16(const void* sbase, unsigned voff) {
    u32x4 r;
    asm volatile("global_load_dwordx4 %0, %1, %2 offset:%3" : "=v"(r) : "v"(voff), "s"(sbase), "i"(IMM) : "memory");
    return r;
}
template <int IMM> __device__ __forceinline__ u32x2 gld8(const void* sbase, unsigned voff) {
    u32x2 r;
    asm volatile("global_load_dwordx2 %0, %1, %2 offset:%3" : "=v"(r) : "v"(voff), "s"(sbase), "i"(IMM) : "memory");
    return r;
}
template <int N> __device__ __forceinline__ void vm_wait2(u32x4& a, u32x4& b) {
    asm volatile("s_waitcnt vmcnt(%2)" : "+v"(a), "+v"(b) : "i"(N) : "memory");
}
template <int N> __device__ __forceinline__ void vm_wait8(u32x4 (&a)[4], u32x4 (&b)[4]) {
    asm volatile("s_waitcnt vmcnt(%8)"
                 : "+v"(a[0]), "+v"(a[1]), "+v"(a[2]), "+v"(a[3]), "+v"(b[0]), "+v"(b[1]), "+v"(b[2]), "+v"(b[3])
                 : "i"(N) : "memory");
}
template <int N> __device__ __forceinline__ void vm_wait2(u32x2& a, u32x2& b) {
    asm volatile("s_waitcnt vmcnt(%2)" : "+v"(a), "+v"(b) : "i"(N) : "memory");
}

constexpr int PACK_GU_THREADS = 2 * 64 * 688;
constexpr int PACK_D_THREADS  = 172 * 256;
constexpr int PACK_BLOCKS = (PACK_GU_THREADS + PACK_D_THREADS) / 256;

__device__ __forceinline__ void pack_item(const u16* __restrict__ src, int N, int ko, int np, u32x4* __restrict__ dst0) {
    unsigned w[8];
#pragma unroll
    for (int j = 0; j < 8; ++j) w[j] = *reinterpret_cast<const unsigned*>(src + (size_t)(8 * ko + j) * N + 2 * np);
    u32x4 o0, o1;
#pragma unroll
    for (int c = 0; c < 4; ++c) {
        o0[c] = (w[2 * c] & 0xffffu) | (w[2 * c + 1] << 16);
        o1[c] = (w[2 * c] >> 16) | (w[2 * c + 1] & 0xffff0000u);
    }
    dst0[0] = o0;
    dst0[1] = o1;
}

__global__ __launch_bounds__(256) void prep_kernel(const u16* __restrict__ k, const u16* __restrict__ v,
                                                   const u16* __restrict__ shift_k,
                                                   const u16* __restrict__ wg, const u16* __restrict__ wu,
                                                   const u16* __restrict__ wd,
                                                   const u16* __restrict__ x0,
                                                   u16* __restrict__ kvR, u32x4* __restrict__ Wgu,
                                                   u32x4* __restrict__ Wd, float* __restrict__ csum,
                                                   f32x2* __restrict__ part0, u32x2* __restrict__ x0t) {
    __shared__ unsigned tile[64 * 33];
    const int t = threadIdx.x;
    if (blockIdx.x < 256) {
        const int st = blockIdx.x >> 3, dt = blockIdx.x & 7;
        const int s0 = st * 64, d0 = dt * 64;
        const int c8 = t & 7, r = t >> 3;
        const int sA = s0 + 2 * r;
        const int dd = d0 + 8 * c8;
        u32x4 kr[5], vr[2], wv[4];
#pragma unroll
        for (int i = 0; i < 5; ++i) {
            const int s = sA - 3 + i;
            u32x4 z = {0u, 0u, 0u, 0u};
            kr[i] = (s >= 0) ? *reinterpret_cast<const u32x4*>(k + (size_t)s * D_ + dd) : z;
        }
#pragma unroll
        for (int i = 0; i < 2; ++i) vr[i] = *reinterpret_cast<const u32x4*>(v + (size_t)(sA + i) * D_ + dd);
#pragma unroll
        for (int i = 0; i < 4; ++i) wv[i] = *reinterpret_cast<const u32x4*>(shift_k + (size_t)dd * 4 + 8 * i);
#pragma unroll
        for (int j = 0; j < 8; ++j) {
            const unsigned wa = wv[j >> 1][(j & 1) * 2], wb = wv[j >> 1][(j & 1) * 2 + 1];
            const float w0 = bflo(wa), w1 = bfhi(wa), w2 = bflo(wb), w3 = bfhi(wb);
            float kk[5];
#pragma unroll
            for (int i = 0; i < 5; ++i) kk[i] = (j & 1) ? bfhi(kr[i][j >> 1]) : bflo(kr[i][j >> 1]);
            float res[2];
#pragma unroll
            for (int q = 0; q < 2; ++q) {
                float acc = rbf(kk[q] * w3);
                acc = rbf(acc + rbf(kk[q + 1] * w2));
                acc = rbf(acc + rbf(kk[q + 2] * w1));
                acc = rbf(acc + rbf(kk[q + 3] * w0));
                const float vv = (j & 1) ? bfhi(vr[q][j >> 1]) : bflo(vr[q][j >> 1]);
                res[q] = acc * vv;
            }
            tile[(8 * c8 + j) * 33 + (31 - r)] = pk2(res[1], res[0]);
        }
        __syncthreads();
#pragma unroll
        for (int i = 0; i < 2; ++i) {
            const int idx = t + 256 * i;
            const int d = idx >> 3, q = idx & 7;
            u32x4 o;
#pragma unroll
            for (int c = 0; c < 4; ++c) o[c] = tile[d * 33 + 4 * q + c];
            *reinterpret_cast<u32x4*>(kvR + (size_t)(d0 + d) * S_ + (S_ - 64 - s0) + 8 * q) = o;
        }
    } else if (blockIdx.x >= 256 + PACK_BLOCKS + 86) {
        const int r0 = (blockIdx.x - (256 + PACK_BLOCKS + 86)) * 16;
        const int row = t & 15;
        f32x2* sp = reinterpret_cast<f32x2*>(tile);
        u32x4 a[4];
#pragma unroll
        for (int i = 0; i < 4; ++i) a[i] = *reinterpret_cast<const u32x4*>(x0 + (size_t)(r0 + row) * D_ + 8 * ((t >> 4) + 16 * i));
#pragma unroll
        for (int i = 0; i < 4; ++i) {
            const int o = (t >> 4) + 16 * i;
            u32x2 lo = {a[i][0], a[i][1]}, hi = {a[i][2], a[i][3]};
            x0t[(size_t)(2 * o) * S_ + r0 + row] = lo;
            x0t[(size_t)(2 * o + 1) * S_ + r0 + row] = hi;
            float sm = 0.f, sq = 0.f;
#pragma unroll
            for (int j = 0; j < 4; ++j) {
                const float l0 = bflo(a[i][j]), h0 = bfhi(a[i][j]);
                sm += l0 + h0;
                sq += l0 * l0 + h0 * h0;
            }
            f32x2 e = {sm, sq};
            sp[row * 64 + o] = e;
        }
        __syncthreads();
        if (t < 128) {
            const int rr = t >> 3, g = t & 7;
            f32x2 e = sp[rr * 64 + 8 * g];
#pragma unroll
            for (int j = 1; j < 8; ++j) e += sp[rr * 64 + 8 * g + j];
            part0[(size_t)(r0 + rr) * 8 + g] = e;
        }
    } else if (blockIdx.x >= 256 + PACK_BLOCKS) {
        const int cbk = blockIdx.x - (256 + PACK_BLOCKS);
        const int m = cbk / 43, f0 = (cbk % 43) * 32;
        const u16* src = m ? wu : wg;
        const int np = t & 15, kg = t >> 4;
        unsigned wv[32];
#pragma unroll
        for (int j = 0; j < 32; ++j) wv[j] = *reinterpret_cast<const unsigned*>(src + (size_t)(32 * kg + j) * F_ + f0 + 2 * np);
        float s0 = 0.f, s1 = 0.f;
#pragma unroll
        for (int j = 0; j < 32; ++j) { s0 += bflo(wv[j]); s1 += bfhi(wv[j]); }
        float* red = reinterpret_cast<float*>(tile);
        red[kg * 33 + 2 * np] = s0;
        red[kg * 33 + 2 * np + 1] = s1;
        __syncthreads();
        if (t < 32) {
            float a = 0.f;
#pragma unroll
            for (int j = 0; j < 16; ++j) a += red[j * 33 + t];
            csum[m * F_ + f0 + t] = a;
        }
    } else {
        int id = (blockIdx.x - 256) * 256 + t;
        if (id < PACK_GU_THREADS) {
            const int np = id % 688;
            const int rest = id / 688;
            const int ko = rest & 63, m = rest >> 6;
            const int n = 2 * np;
            const int ft = n >> 4, ks = ko >> 2, g = ko & 3;
            pack_item(m ? wu : wg, F_, ko, np, Wgu + ((size_t)((ft * KS1 + ks) * 2 + m) * 64 + g * 16 + (n & 15)));
        } else {
            id -= PACK_GU_THREADS;
            const int np = id & 255;
            const int ko = id >> 8;
            const int n = 2 * np;
            const int nt = n >> 4, ks = ko >> 2, g = ko & 3;
            pack_item(wd, D_, ko, np, Wd + ((size_t)(nt * KS2 + ks) * 64 + g * 16 + (n & 15)));
        }
    }
}

constexpr int CV_C = 4160;
constexpr int CV_U = 4 * CV_C;
constexpr int CV_CH = CV_U + 4608;
constexpr int CV_Y = 4 * CV_CH;

template <int OFF> __device__ __forceinline__ u32x2 lds8(unsigned addr) {
    u32x2 r;
    asm volatile("ds_read_b64 %0, %1 offset:%2" : "=v"(r) : "v"(addr), "i"(OFF) : "memory");
    return r;
}
template <int OFF> __device__ __forceinline__ u32x4 lds16(unsigned addr) {
    u32x4 r;
    asm volatile("ds_read_b128 %0, %1 offset:%2" : "=v"(r) : "v"(addr), "i"(OFF) : "memory");
    return r;
}
__device__ __forceinline__ void lgkm_wait(u32x2 (&a)[16], u32x4 (&b)[8]) {
    asm volatile("s_waitcnt lgkmcnt(0)"
                 : "+v"(a[0]), "+v"(a[1]), "+v"(a[2]), "+v"(a[3]), "+v"(a[4]), "+v"(a[5]), "+v"(a[6]), "+v"(a[7]),
                   "+v"(a[8]), "+v"(a[9]), "+v"(a[10]), "+v"(a[11]), "+v"(a[12]), "+v"(a[13]), "+v"(a[14]), "+v"(a[15]),
                   "+v"(b[0]), "+v"(b[1]), "+v"(b[2]), "+v"(b[3]), "+v"(b[4]), "+v"(b[5]), "+v"(b[6]), "+v"(b[7])
                 :: "memory");
}
template <int BLK, int GBLK>
__device__ __forceinline__ void conv_load(unsigned fa, unsigned ga, u32x2 (&F)[16], u32x4 (&G)[8]) {
#define CVL(i) F[2 * (i)] = lds8<64 * (8 * BLK + (i))>(fa); F[2 * (i) + 1] = lds8<64 * (8 * BLK + (i)) + 8>(fa); \
               G[i] = lds16<64 * (8 * (GBLK + 7) + (i))>(ga);
    CVL(0) CVL(1) CVL(2) CVL(3) CVL(4) CVL(5) CVL(6) CVL(7)
#undef CVL
}
__device__ __forceinline__ u32x4 cat(u32x2 a, u32x2 b) { u32x4 r = {a[0], a[1], b[0], b[1]}; return r; }

template <int N, int T>
__device__ __forceinline__ void conv_step(unsigned fa, unsigned ga, u32x2 (&Fr)[3][16], u32x4 (&Gr)[2][8], f32x4& acc1,
                                          f32x4& acc0) {
    if (T >= 0) lgkm_wait(Fr[T % 3], Gr[T & 1]);
    if (T + 1 <= N + 1) conv_load<T + 1, T - N>(fa, ga, Fr[(T + 1) % 3], Gr[(T + 1) & 1]);
    __builtin_amdgcn_sched_barrier(0);
    if (T >= 0) {
#pragma unroll
        for (int i = 0; i < 8; ++i) {
            acc1 = mfma16(cat(Fr[T % 3][2 * i], Fr[T % 3][2 * i + 1]), Gr[T & 1][i], acc1);
            if (T >= 1) acc0 = mfma16(cat(Fr[(T + 2) % 3][2 * i], Fr[(T + 2) % 3][2 * i + 1]), Gr[T & 1][i], acc0);
        }
    }
    __builtin_amdgcn_sched_barrier(0);
    if constexpr (T < N + 1) conv_step<N, T + 1>(fa, ga, Fr, Gr, acc1, acc0);
}
template <int N>
__device__ __forceinline__ void conv_task(unsigned fa, unsigned ga, f32x4& acc1, f32x4& acc0) {
    u32x2 Fr[3][16];
    u32x4 Gr[2][8];
    conv_step<N, -1>(fa, ga, Fr, Gr, acc1, acc0);
}

__device__ __forceinline__ unsigned sh16(unsigned lo, unsigned hi) { return (lo >> 16) | (hi << 16); }

__global__ __launch_bounds__(256) void conv_kernel(const u16* __restrict__ kvR, const u16* __restrict__ ssm_k,
                                                   const u16* __restrict__ ssm_D, u16* __restrict__ Y4) {
    __shared__ __attribute__((aligned(16))) char smem[CV_Y + 8704];
    const int t = threadIdx.x, l = t & 63;
    const int w = __builtin_amdgcn_readfirstlane(t >> 6);
    const int cq = blockIdx.x >> 1, half = blockIdx.x & 1;
    const int ch = 4 * cq + w;
    char* base = smem + w * CV_CH;
    const float Dv = bflo((unsigned)ssm_D[ch]);
    {
        const u16* hp = ssm_k + (size_t)ch * S_;
        const u16* up = kvR + (size_t)ch * S_;
        u32x4 hv[4], uv[4];
        u32x2 hx[4];
#pragma unroll
        for (int i = 0; i < 4; ++i) {
            const int m = l + 64 * i;
            hv[i] = *reinterpret_cast<const u32x4*>(hp + 8 * m);
            uv[i] = *reinterpret_cast<const u32x4*>(up + 8 * m);
            const u32x2 zz = {0u, 0u};
            hx[i] = (m < 255) ? *reinterpret_cast<const u32x2*>(hp + 8 * m + 8) : zz;
        }
        const u32x4 z = {0u, 0u, 0u, 0u};
        if (l < 32) *reinterpret_cast<u32x4*>(base + CV_U + 4096 + 16 * l) = z;
        if (l < 4) *reinterpret_cast<u32x4*>(base + l * CV_C) = z;
        if (l == 0) {
            const unsigned h01 = hv[0][0], h23 = hv[0][1];
            u32x4 c0 = z, c1 = z, c2 = z, c3 = z;
            c0[3] = h01 << 16;
            c1[3] = h01;
            c2[2] = h01 << 16; c2[3] = sh16(h01, h23);
            c3[2] = h01;       c3[3] = h23;
            *reinterpret_cast<u32x4*>(base + 0 * CV_C + 16) = c0;
            *reinterpret_cast<u32x4*>(base + 1 * CV_C + 16) = c1;
            *reinterpret_cast<u32x4*>(base + 2 * CV_C + 16) = c2;
            *reinterpret_cast<u32x4*>(base + 3 * CV_C + 16) = c3;
        }
#pragma unroll
        for (int i = 0; i < 4; ++i) {
            const int m = l + 64 * i;
            const unsigned d0 = hv[i][0], d1 = hv[i][1], d2 = hv[i][2], d3 = hv[i][3], d4 = hx[i][0], d5 = hx[i][1];
            const unsigned e0 = sh16(d0, d1), e1 = sh16(d1, d2), e2 = sh16(d2, d3), e3 = sh16(d3, d4), e4 = sh16(d4, d5);
            u32x4 c0 = {e0, e1, e2, e3}, c1 = {d1, d2, d3, d4}, c2 = {e1, e2, e3, e4}, c3 = {d2, d3, d4, d5};
            *reinterpret_cast<u32x4*>(base + 0 * CV_C + 32 + 16 * m) = c0;
            *reinterpret_cast<u32x4*>(base + 1 * CV_C + 32 + 16 * m) = c1;
            *reinterpret_cast<u32x4*>(base + 2 * CV_C + 32 + 16 * m) = c2;
            *reinterpret_cast<u32x4*>(base + 3 * CV_C + 32 + 16 * m) = c3;
            *reinterpret_cast<u32x4*>(base + CV_U + 16 * m) = uv[i];
        }
    }
    __syncthreads();

    const int o0 = (l & 15) + 8 * (l >> 4);
    const unsigned fp = (unsigned)(size_t)(base + (l & 3) * CV_C + 2 * (o0 & ~3));
    const unsigned gp = (unsigned)(size_t)(base + CV_U + 2 * (2032 - 16 * (l & 15) + 8 * (l >> 4)) - 64 * 56);
    f32x4 a0 = {0.f, 0.f, 0.f, 0.f}, a1 = a0, a2 = a0, a3 = a0;
    int n0, n1, n2, n3;
    if (half == 0) {
        n0 = 7; n1 = 6; n2 = 1; n3 = 0;
        conv_task<6>(fp, gp, a0, a1);
        conv_task<0>(fp, gp, a2, a3);
    } else {
        n0 = 5; n1 = 4; n2 = 3; n3 = 2;
        conv_task<4>(fp, gp, a0, a1);
        conv_task<2>(fp, gp, a2, a3);
    }
    u16* yb = reinterpret_cast<u16*>(smem + CV_Y);
    const int sl = 16 * (l & 15) + 4 * (l >> 4);
    const int slp = sl + (l & 15);
#define EPI(acc, n, i)                                                                        \
    {                                                                                         \
        const int s = 256 * (n) + sl;                                                         \
        const u32x2 uu = *reinterpret_cast<const u32x2*>(base + CV_U + 2 * (2044 - s));       \
        const float u0 = bfhi(uu[1]), u1 = bflo(uu[1]), u2 = bfhi(uu[0]), u3 = bflo(uu[0]);   \
        const unsigned c01 = pk2(acc[0], acc[1]), c23 = pk2(acc[2], acc[3]);                  \
        const unsigned k01 = pk2(u0 * Dv, u1 * Dv), k23 = pk2(u2 * Dv, u3 * Dv);              \
        const unsigned p01 = pk2(bflo(c01) + bflo(k01), bfhi(c01) + bfhi(k01));               \
        const unsigned p23 = pk2(bflo(c23) + bflo(k23), bfhi(c23) + bfhi(k23));               \
        u16* q = yb + ((i) * 272 + slp) * 4 + w;                                              \
        q[0] = (u16)p01; q[4] = (u16)(p01 >> 16); q[8] = (u16)p23; q[12] = (u16)(p23 >> 16);  \
    }
    EPI(a0, n0, 0) EPI(a1, n1, 1) EPI(a2, n2, 2) EPI(a3, n3, 3)
    __syncthreads();
    {
        const u32x2* yr = reinterpret_cast<const u32x2*>(smem + CV_Y) + t + (t >> 4);
        u32x2* dst = reinterpret_cast<u32x2*>(Y4) + (size_t)cq * S_;
        dst[256 * n0 + t] = yr[0];
        dst[256 * n1 + t] = yr[272];
        dst[256 * n2 + t] = yr[544];
        dst[256 * n3 + t] = yr[816];
    }
}

__device__ __forceinline__ float rowsum8(float v) {
    v += __builtin_bit_cast(float, __builtin_amdgcn_update_dpp(0, __builtin_bit_cast(int, v), 0x128, 0xf, 0xf, false));
    {
        const unsigned u = __builtin_bit_cast(unsigned, v);
        auto r = __builtin_amdgcn_permlane16_swap(u, u, false, false);
        v = __builtin_bit_cast(float, (unsigned)r[0]) + __builtin_bit_cast(float, (unsigned)r[1]);
    }
    {
        const unsigned u = __builtin_bit_cast(unsigned, v);
        auto r = __builtin_amdgcn_permlane32_swap(u, u, false, false);
        v = __builtin_bit_cast(float, (unsigned)r[0]) + __builtin_bit_cast(float, (unsigned)r[1]);
    }
    return v;
}
__device__ __forceinline__ f32x2 unpk(unsigned u) { f32x2 r = {bflo(u), bfhi(u)}; return r; }

__device__ __forceinline__ void g1_epilogue(const f32x4 (&ag)[4], const f32x4 (&au)[4], int rb, int ft, int l,
                                            u32x2* __restrict__ hpk, const f32x2* st2, u32x4 cgu, u32x4 cuu) {
    const int ks2 = ft >> 1;
    const int slot = (2 * (ft & 1) + (l >> 5)) * 16 + (l & 15);
    const f32x4 cg = __builtin_bit_cast(f32x4, cgu), cu = __builtin_bit_cast(f32x4, cuu);
    float sa[4], sb[4];
#pragma unroll
    for (int rt = 0; rt < 4; ++rt) {
        const f32x4 pp = *reinterpret_cast<const f32x4*>(st2 + 2 * (16 * rt + (l & 15)));
        const float mean2 = (pp[0] + pp[2]) * (1.0f / 512.0f);
        const float inv2 = __builtin_amdgcn_rsqf(fmaxf((pp[1] + pp[3]) * (1.0f / 512.0f) - mean2 * mean2, 0.f) + 1e-5f);
        sa[rt] = inv2;
        sb[rt] = -mean2 * inv2;
    }
    u32x2* dst = hpk + ((size_t)(rb * 4 * KS2 + ks2) * 64 + slot) * 2 + ((l >> 4) & 1);
#pragma unroll
    for (int rt = 0; rt < 4; ++rt) {
        const f32x4 g4 = ag[rt] * sa[rt] + cg * sb[rt];
        const f32x4 u4 = au[rt] * sa[rt] + cu * sb[rt];
        const unsigned g01 = pk2(g4[0], g4[1]), g23 = pk2(g4[2], g4[3]);
        const unsigned u01 = pk2(u4[0], u4[1]), u23 = pk2(u4[2], u4[3]);
        const float g0 = bflo(g01), g1 = bfhi(g01), g2 = bflo(g23), g3 = bfhi(g23);
        const float e0 = __expf(-g0), e1 = __expf(-g1), e2 = __expf(-g2), e3 = __expf(-g3);
        const float r0 = __builtin_amdgcn_rcpf(1.0f + e0), r1 = __builtin_amdgcn_rcpf(1.0f + e1);
        const float r2 = __builtin_amdgcn_rcpf(1.0f + e2), r3 = __builtin_amdgcn_rcpf(1.0f + e3);
        const unsigned s01 = pk2(g0 * r0, g1 * r1), s23 = pk2(g2 * r2, g3 * r3);
        u32x2 o;
        o[0] = pk2(bflo(s01) * bflo(u01), bfhi(s01) * bfhi(u01));
        o[1] = pk2(bflo(s23) * bflo(u23), bfhi(s23) * bfhi(u23));
        dst[(size_t)rt * KS2 * 128] = o;
    }
}

constexpr int G1_WIN = 6;
__device__ constexpr int g1_ks(int t) { return t < 4 ? t : t < 8 ? t + 4 : t < 12 ? t - 4 : t; }
__device__ constexpr int g1_younger(int t) { return 2 * ((t - 1 + G1_WIN < 15 ? t - 1 + G1_WIN : 15) - t); }
#define G1_LOADW(t)                                                                                           \
    {                                                                                                         \
        wf[(t) % G1_WIN][0] = gld16<((2 * g1_ks(t)) & 3) * 1024>(wbase, wo + ((2 * g1_ks(t)) >> 2) * 4096);     \
        wf[(t) % G1_WIN][1] = gld16<((2 * g1_ks(t) + 1) & 3) * 1024>(wbase, wo + ((2 * g1_ks(t)) >> 2) * 4096); \
    }
#define G1_STEP(t)                                                                            \
    {                                                                                         \
        vm_wait2<g1_younger(t)>(wf[(t) % G1_WIN][0], wf[(t) % G1_WIN][1]);                    \
        _Pragma("unroll") for (int rt = 0; rt < 4; ++rt) {                                    \
            const u32x4 bb = At[(rt * KS1 + g1_ks(t)) * 64 + l];                              \
            ag[rt] = mfma16(wf[(t) % G1_WIN][0], bb, ag[rt]);                                 \
            au[rt] = mfma16(wf[(t) % G1_WIN][1], bb, au[rt]);                                 \
        }                                                                                     \
        if ((t) + G1_WIN < 16) G1_LOADW((t) + G1_WIN)                                         \
    }
#define G1_STEPS8(b)                                                                          \
    G1_STEP(b + 0) G1_STEP(b + 1) G1_STEP(b + 2) G1_STEP(b + 3) G1_STEP(b + 4) G1_STEP(b + 5) \
    G1_STEP(b + 6) G1_STEP(b + 7)

template <bool LATE>
__device__ __forceinline__ void g1_mfma_role(const u32x4* At, const f32x2* st2, const u32x4* wbase,
                                             const float* csum, int l, int rb, int ft, u32x2* __restrict__ hpk) {
    const unsigned wo = 16 * l;
    u32x4 wf[G1_WIN][2];
    const unsigned co = (16 * ft + 4 * (l >> 4)) * 4;
    u32x4 cs0 = gld16<0>(csum, co);
    u32x4 cs1 = gld16<0>(csum, co + F_ * 4);
    G1_LOADW(0) G1_LOADW(1) G1_LOADW(2) G1_LOADW(3)
    __syncthreads();
    G1_LOADW(4) G1_LOADW(5)
    f32x4 ag[4], au[4];
#pragma unroll
    for (int r = 0; r < 4; ++r) { ag[r] = (f32x4){0.f, 0.f, 0.f, 0.f}; au[r] = ag[r]; }
    G1_STEPS8(0)
    if (!LATE) __syncthreads();
    G1_STEPS8(8)
    vm_wait2<0>(cs0, cs1);
    g1_epilogue(ag, au, rb, ft, l, hpk, st2, cs0, cs1);
}

__global__ __launch_bounds__(1024, 4) void g1_kernel(const u32x2* __restrict__ x, const f32x2* __restrict__ part,
                                                     const u32x2* __restrict__ Y4, const u32x4* __restrict__ Wgu,
                                                     const float* __restrict__ csum, u32x2* __restrict__ x1out,
                                                     u32x2* __restrict__ hpk) {
    __shared__ u32x4 At[4 * KS1 * 64 + 64];
    f32x2* st2 = reinterpret_cast<f32x2*>(At + 4 * KS1 * 64);
    const int b = blockIdx.x;
    const int rb = (b & 7) * 4 + (b >> 6), cb = (b >> 3) & 7;
    const int l = threadIdx.x & 63;
    const int w = __builtin_amdgcn_readfirstlane(threadIdx.x >> 6);
    const int ntl = cb < 6 ? 11 : 10;
    const int ft0 = cb < 6 ? 11 * cb : 66 + 10 * (cb - 6);
    if (w < 8) {
        const int ip = l & 7, g = l >> 3;
        const int rt = w >> 1, dh = w & 1;
        const int row = rb * 64 + 16 * rt + 2 * ip;
        u32x4 xv[8], yv[8];
        u32x2 pa = gld8<0>(part, (unsigned)(row * 8 + g) * 8);
        u32x2 pb = gld8<64>(part, (unsigned)(row * 8 + g) * 8);
        const unsigned o0 = (unsigned)((64 * dh + g) * S_ + row) * 8;
#pragma unroll
        for (int c = 0; c < 8; ++c) {
            xv[c] = gld16<0>(x, o0 + c * (8 * S_ * 8));
            yv[c] = gld16<0>(Y4, o0 + c * (8 * S_ * 8));
        }
        vm_wait2<16>(pa, pb);
        const float meanA = rowsum8(__builtin_bit_cast(float, (unsigned)pa[0])) * (1.0f / 512.0f);
        const float msqA = rowsum8(__builtin_bit_cast(float, (unsigned)pa[1])) * (1.0f / 512.0f);
        const float invA = 1.0f / sqrtf(fmaxf(msqA - meanA * meanA, 0.f) + 1e-5f);
        const float meanB = rowsum8(__builtin_bit_cast(float, (unsigned)pb[0])) * (1.0f / 512.0f);
        const float msqB = rowsum8(__builtin_bit_cast(float, (unsigned)pb[1])) * (1.0f / 512.0f);
        const float invB = 1.0f / sqrtf(fmaxf(msqB - meanB * meanB, 0.f) + 1e-5f);
        const f32x2 iA = {invA, invA}, bA = {-meanA * invA, -meanA * invA};
        const f32x2 iB = {invB, invB}, bB = {-meanB * invB, -meanB * invB};
        f32x2 sa = {0.f, 0.f}, sb = sa, qa = sa, qb = sa;
        u32x2* At2 = reinterpret_cast<u32x2*>(At);
#define G1_TRANS(c)                                                                                   \
        {                                                                                             \
            vm_wait2<14 - 2 * (c)>(xv[c], yv[c]);                                                     \
            u32x4 o;                                                                                  \
            _Pragma("unroll") for (int j = 0; j < 4; ++j) {                                           \
                const f32x2 xx = unpk(xv[c][j]);                                                      \
                const f32x2 yy = unpk(yv[c][j]);                                                      \
                const f32x2 q = (j < 2) ? (xx * iA + bA) : (xx * iB + bB);                            \
                const f32x2 r = xx + q * yy;                                                          \
                o[j] = pk2(r[0], r[1]);                                                               \
                if (j < 2) { sa += r; qa += r * r; } else { sb += r; qb += r * r; }                   \
            }                                                                                         \
            const int unit = (rt * KS1 + 8 * dh + (c)) * 64 + (g >> 1) * 16 + 2 * ip;                 \
            u32x2 oa = {o[0], o[1]}, ob = {o[2], o[3]};                                               \
            At2[2 * unit + (g & 1)] = oa;                                                             \
            At2[2 * (unit + 1) + (g & 1)] = ob;                                                       \
            xv[c] = o;                                                                                \
        }
        G1_TRANS(0) G1_TRANS(1) G1_TRANS(2) G1_TRANS(3)
        __syncthreads();
        G1_TRANS(4) G1_TRANS(5) G1_TRANS(6) G1_TRANS(7)
#pragma unroll
        for (int c = 0; c < 8; ++c)
            if ((8 * dh + c) >> 1 == cb)
                *reinterpret_cast<u32x4*>(x1out + (size_t)(64 * dh + 8 * c + g) * S_ + row) = xv[c];
        const float s_a = rowsum8(sa[0] + sa[1]), q_a = rowsum8(qa[0] + qa[1]);
        const float s_b = rowsum8(sb[0] + sb[1]), q_b = rowsum8(qb[0] + qb[1]);
        if (g == 0) {
            f32x2 ea = {s_a, q_a}, eb = {s_b, q_b};
            st2[2 * (16 * rt + 2 * ip) + dh] = ea;
            st2[2 * (16 * rt + 2 * ip + 1) + dh] = eb;
        }
        if (w + 8 < ntl) g1_mfma_role<true>(At, st2, Wgu + (size_t)(ft0 + 8 + w) * (KS1 * 2 * 64), csum, l, rb, ft0 + 8 + w, hpk);
        else __syncthreads();
    } else {
        g1_mfma_role<false>(At, st2, Wgu + (size_t)(ft0 + w - 8) * (KS1 * 2 * 64), csum, l, rb, ft0 + w - 8, hpk);
    }
}

__device__ __forceinline__ void g2_main(const u32x4* hbase, const u32x4* wbase, int l, bool six, f32x4 (&acc)[4][4]) {
    u32x4 af[5][4], bf[5][4];
    unsigned vo[4];
#pragma unroll
    for (int i = 0; i < 4; ++i) vo[i] = 16 * l + i * (KS2 * 1024);
#define G2_LOAD(st)                                                                                   \
    _Pragma("unroll") for (int i = 0; i < 4; ++i) {                                                   \
        af[(st) % 5][i] = gld16<((st) & 3) * 1024>(wbase, vo[i] + ((st) >> 2) * 4096);                \
        bf[(st) % 5][i] = gld16<((st) & 3) * 1024>(hbase, vo[i] + ((st) >> 2) * 4096);                \
    }
#define G2_STEP(st, nwait)                                                                            \
    {                                                                                                 \
        vm_wait8<nwait>(af[(st) % 5], bf[(st) % 5]);                                                  \
        _Pragma("unroll") for (int i = 0; i < 4; ++i)                                                 \
            _Pragma("unroll") for (int j = 0; j < 4; ++j)                                             \
                acc[i][j] = mfma16(af[(st) % 5][i], bf[(st) % 5][j], acc[i][j]);                      \
        __builtin_amdgcn_sched_barrier(0);                                                            \
    }
    G2_LOAD(0) G2_LOAD(1) G2_LOAD(2) G2_LOAD(3) G2_LOAD(4)
    G2_STEP(0, 32)
    if (six) { G2_LOAD(5) }
    G2_STEP(1, 24) G2_STEP(2, 16) G2_STEP(3, 8) G2_STEP(4, 0)
    if (six) { G2_STEP(5, 0) }
}

__device__ __forceinline__ float quadsum(float v) {
    {
        const unsigned u = __builtin_bit_cast(unsigned, v);
        auto r = __builtin_amdgcn_permlane16_swap(u, u, false, false);
        v = __builtin_bit_cast(float, (unsigned)r[0]) + __builtin_bit_cast(float, (unsigned)r[1]);
    }
    {
        const unsigned u = __builtin_bit_cast(unsigned, v);
        auto r = __builtin_amdgcn_permlane32_swap(u, u, false, false);
        v = __builtin_bit_cast(float, (unsigned)r[0]) + __builtin_bit_cast(float, (unsigned)r[1]);
    }
    return v;
}

__global__ __launch_bounds__(512, 2) void g2_kernel(const u32x4* __restrict__ hpk, const u32x4* __restrict__ Wd,
                                                    const u32x2* __restrict__ xin, u32x2* __restrict__ xout,
                                                    f32x2* __restrict__ part, int STATS) {
    __shared__ f32x4 red[8 * 16 * 64 + 128];
    f32x2* sred = reinterpret_cast<f32x2*>(red + 8 * 16 * 64);
    const int b = blockIdx.x;
    const int rb = (b & 7) * 4 + (b >> 6), cb = (b >> 3) & 7;
    const int l = threadIdx.x & 63;
    const int w = __builtin_amdgcn_readfirstlane(threadIdx.x >> 6);
    const int start = 5 * w + (w < 3 ? w : 3);
    const u32x4* hbase = hpk + ((size_t)(rb * 4) * KS2 + start) * 64;
    const u32x4* wbase = Wd + ((size_t)(cb * 4) * KS2 + start) * 64;
    const int n = cb * 64 + 16 * (w >> 1) + 4 * (l >> 4);
    const int s0 = rb * 64 + 32 * (w & 1) + (l & 15);
    const unsigned xo = (unsigned)((n >> 2) * S_ + s0) * 8;
    u32x2 xr0 = gld8<0>(xin, xo), xr1 = gld8<128>(xin, xo);
    f32x4 acc[4][4];
#pragma unroll
    for (int i = 0; i < 4; ++i)
#pragma unroll
        for (int j = 0; j < 4; ++j) acc[i][j] = (f32x4){0.f, 0.f, 0.f, 0.f};
    g2_main(hbase, wbase, l, w < 3, acc);
#pragma unroll
    for (int i = 0; i < 4; ++i)
#pragma unroll
        for (int j = 0; j < 4; ++j) red[(w * 16 + i * 4 + j) * 64 + l] = acc[i][j];
    __syncthreads();
    vm_wait2<0>(xr0, xr1);
#pragma unroll
    for (int q = 0; q < 2; ++q) {
        const int tile = 2 * w + q;
        const u32x2 xr = q ? xr1 : xr0;
        f32x4 sum = red[tile * 64 + l];
#pragma unroll
        for (int p = 1; p < 8; ++p) sum += red[(p * 16 + tile) * 64 + l];
        u32x2 o;
        o[0] = pk2(bflo(xr[0]) + rbf(sum[0]), bfhi(xr[0]) + rbf(sum[1]));
        o[1] = pk2(bflo(xr[1]) + rbf(sum[2]), bfhi(xr[1]) + rbf(sum[3]));
        if (STATS) xout[(size_t)(n >> 2) * S_ + s0 + 16 * q] = o;
        else xout[((size_t)(s0 + 16 * q) * D_ + n) >> 2] = o;
        if (STATS) {
            const float v0 = bflo(o[0]), v1 = bfhi(o[0]), v2 = bflo(o[1]), v3 = bfhi(o[1]);
            const float sm = quadsum((v0 + v1) + (v2 + v3));
            const float sq = quadsum((v0 * v0 + v1 * v1) + (v2 * v2 + v3 * v3));
            if (l < 16) { f32x2 e = {sm, sq}; sred[(w >> 1) * 64 + 32 * (w & 1) + 16 * q + l] = e; }
        }
    }
    if (STATS) {
        __syncthreads();
        if (threadIdx.x < 64) {
            const f32x2 e = (sred[threadIdx.x] + sred[64 + threadIdx.x]) + (sred[128 + threadIdx.x] + sred[192 + threadIdx.x]);
            part[(size_t)(rb * 64 + threadIdx.x) * 8 + cb] = e;
        }
    }
}

extern "C" void kernel_launch(void* const* d_in, const int* in_sizes, int n_in,
                              void* d_out, int out_size, void* d_ws, size_t ws_size,
                              hipStream_t stream) {
    const u16* q       = (const u16*)d_in[0];
    const u16* k       = (const u16*)d_in[1];
    const u16* v       = (const u16*)d_in[2];
    const u16* shift_k = (const u16*)d_in[3];
    const u16* ssm_k   = (const u16*)d_in[4];
    const u16* ssm_D   = (const u16*)d_in[5];
    const u16* w_gate  = (const u16*)d_in[7];
    const u16* w_up    = (const u16*)d_in[8];
    const u16* w_down  = (const u16*)d_in[9];
    u16* out = (u16*)d_out;

    char* ws = (char*)d_ws;
    const size_t MB2 = (size_t)S_ * D_ * 2;
    u16*   kvR = (u16*)(ws);
    u16*   x1  = (u16*)(ws);
    u16*   x3  = (u16*)(ws);
    u16*   Y4  = (u16*)(ws + MB2);
    u32x2* x0t = (u32x2*)(ws + 2 * MB2);
    u16*   x2  = (u16*)(ws + 2 * MB2);
    u32x4* Wgu = (u32x4*)(ws + 3 * MB2);
    u32x4* Wd  = (u32x4*)(ws + 3 * MB2 + (size_t)NFT * KS1 * 2 * 1024);
    u32x4* hpk = (u32x4*)(ws + 3 * MB2 + (size_t)NFT * KS1 * 2 * 1024 + (size_t)32 * KS2 * 1024);
    char* tail = (char*)hpk + (size_t)128 * KS2 * 1024;
    float* csum  = (float*)tail;
    f32x2* part0 = (f32x2*)(tail + 16384);
    f32x2* part2 = (f32x2*)(tail + 16384 + 131072);

    prep_kernel<<<256 + PACK_BLOCKS + 86 + 128, 256, 0, stream>>>(k, v, shift_k, w_gate, w_up, w_down, q, kvR, Wgu, Wd,
                                                                  csum, part0, x0t);
    conv_kernel<<<256, 256, 0, stream>>>(kvR, ssm_k, ssm_D, Y4);
    g1_kernel<<<256, 1024, 0, stream>>>(x0t, part0, (const u32x2*)Y4, Wgu, csum, (u32x2*)x1, (u32x2*)hpk);
    g2_kernel<<<256, 512, 0, stream>>>(hpk, Wd, (const u32x2*)x1, (u32x2*)x2, part2, 1);
    g1_kernel<<<256, 1024, 0, stream>>>((const u32x2*)x2, part2, (const u32x2*)Y4, Wgu, csum, (u32x2*)x3, (u32x2*)hpk);
    g2_kernel<<<256, 512, 0, stream>>>(hpk, Wd, (const u32x2*)x3, (u32x2*)out, part2, 0);
}
```

```cpp
#include <hip/hip_runtime.h>
#include <hip/hip_bf16.h>
#include <stdint.h>

typedef __attribute__((ext_vector_type(8))) __bf16   bf16x8;
typedef __attribute__((ext_vector_type(2))) __bf16   bf16x2;
typedef __attribute__((ext_vector_type(4))) float    f32x4;
typedef __attribute__((ext_vector_type(2))) float    f32x2;
typedef __attribute__((ext_vector_type(4))) unsigned u32x4;
typedef __attribute__((ext_vector_type(2))) unsigned u32x2;
typedef unsigned short u16;

constexpr int S_ = 2048, D_ = 512, F_ = 1376;
constexpr int NFT = 86;
constexpr int KS1 = 16;
constexpr int KS2 = 43;

__device__ __forceinline__ float bflo(unsigned u) { return __builtin_bit_cast(float, u << 16); }
__device__ __forceinline__ float bfhi(unsigned u) { return __builtin_bit_cast(float, u & 0xffff0000u); }
__device__ __forceinline__ unsigned pk2(float lo, float hi) {
    f32x2 v = {lo, hi};
    bf16x2 r = __builtin_convertvector(v, bf16x2);
    return __builtin_bit_cast(unsigned, r);
}
__device__ __forceinline__ float rbf(float x) { return bflo(pk2(x, x)); }
__device__ __forceinline__ f32x4 mfma16(u32x4 a, u32x4 b, f32x4 c) {
    return __builtin_amdgcn_mfma_f32_16x16x32_bf16(__builtin_bit_cast(bf16x8, a), __builtin_bit_cast(bf16x8, b), c, 0, 0, 0);
}

template <int IMM> __device__ __forceinline__ u32x4 gld16(const void* sbase, unsigned voff) {
    u32x4 r;
    asm volatile("global_load_dwordx4 %0, %1, %2 offset:%3" : "=v"(r) : "v"(voff), "s"(sbase), "i"(IMM) : "memory");
    return r;
}
template <int IMM> __device__ __forceinline__ u32x2 gld8(const void* sbase, unsigned voff) {
    u32x2 r;
    asm volatile("global_load_dwordx2 %0, %1, %2 offset:%3" : "=v"(r) : "v"(voff), "s"(sbase), "i"(IMM) : "memory");
    return r;
}
template <int N> __device__ __forceinline__ void vm_wait2(u32x4& a, u32x4& b) {
    asm volatile("s_waitcnt vmcnt(%2)" : "+v"(a), "+v"(b) : "i"(N) : "memory");
}
template <int N> __device__ __forceinline__ void vm_wait8(u32x4 (&a)[4], u32x4 (&b)[4]) {
    asm volatile("s_waitcnt vmcnt(%8)"
                 : "+v"(a[0]), "+v"(a[1]), "+v"(a[2]), "+v"(a[3]), "+v"(b[0]), "+v"(b[1]), "+v"(b[2]), "+v"(b[3])
                 : "i"(N) : "memory");
}
template <int N> __device__ __forceinline__ void vm_wait2(u32x2& a, u32x2& b) {
    asm volatile("s_waitcnt vmcnt(%2)" : "+v"(a), "+v"(b) : "i"(N) : "memory");
}

constexpr int PACK_GU_THREADS = 2 * 64 * 688;
constexpr int PACK_D_THREADS  = 172 * 256;
constexpr int PACK_BLOCKS = (PACK_GU_THREADS + PACK_D_THREADS) / 256;

__device__ __forceinline__ void pack_item(const u16* __restrict__ src, int N, int ko, int np, u32x4* __restrict__ dst0) {
    unsigned w[8];
#pragma unroll
    for (int j = 0; j < 8; ++j) w[j] = *reinterpret_cast<const unsigned*>(src + (size_t)(8 * ko + j) * N + 2 * np);
    u32x4 o0, o1;
#pragma unroll
    for (int c = 0; c < 4; ++c) {
        o0[c] = (w[2 * c] & 0xffffu) | (w[2 * c + 1] << 16);
        o1[c] = (w[2 * c] >> 16) | (w[2 * c + 1] & 0xffff0000u);
    }
    dst0[0] = o0;
    dst0[1] = o1;
}

__global__ __launch_bounds__(256) void prep_kernel(const u16* __restrict__ k, const u16* __restrict__ v,
                                                   const u16* __restrict__ shift_k,
                                                   const u16* __restrict__ wg, const u16* __restrict__ wu,
                                                   const u16* __restrict__ wd,
                                                   const u16* __restrict__ x0,
                                                   u16* __restrict__ kvR, u32x4* __restrict__ Wgu,
                                                   u32x4* __restrict__ Wd, float* __restrict__ csum,
                                                   f32x2* __restrict__ part0, u32x2* __restrict__ x0t) {
    __shared__ unsigned tile[64 * 33];
    const int t = threadIdx.x;
    if (blockIdx.x < 256) {
        const int st = blockIdx.x >> 3, dt = blockIdx.x & 7;
        const int s0 = st * 64, d0 = dt * 64;
        const int c8 = t & 7, r = t >> 3;
        const int sA = s0 + 2 * r;
        const int dd = d0 + 8 * c8;
        u32x4 kr[5], vr[2], wv[4];
#pragma unroll
        for (int i = 0; i < 5; ++i) {
            const int s = sA - 3 + i;
            u32x4 z = {0u, 0u, 0u, 0u};
            kr[i] = (s >= 0) ? *reinterpret_cast<const u32x4*>(k + (size_t)s * D_ + dd) : z;
        }
#pragma unroll
        for (int i = 0; i < 2; ++i) vr[i] = *reinterpret_cast<const u32x4*>(v + (size_t)(sA + i) * D_ + dd);
#pragma unroll
        for (int i = 0; i < 4; ++i) wv[i] = *reinterpret_cast<const u32x4*>(shift_k + (size_t)dd * 4 + 8 * i);
#pragma unroll
        for (int j = 0; j < 8; ++j) {
            const unsigned wa = wv[j >> 1][(j & 1) * 2], wb = wv[j >> 1][(j & 1) * 2 + 1];
            const float w0 = bflo(wa), w1 = bfhi(wa), w2 = bflo(wb), w3 = bfhi(wb);
            float kk[5];
#pragma unroll
            for (int i = 0; i < 5; ++i) kk[i] = (j & 1) ? bfhi(kr[i][j >> 1]) : bflo(kr[i][j >> 1]);
            float res[2];
#pragma unroll
            for (int q = 0; q < 2; ++q) {
                float acc = rbf(kk[q] * w3);
                acc = rbf(acc + rbf(kk[q + 1] * w2));
                acc = rbf(acc + rbf(kk[q + 2] * w1));
                acc = rbf(acc + rbf(kk[q + 3] * w0));
                const float vv = (j & 1) ? bfhi(vr[q][j >> 1]) : bflo(vr[q][j >> 1]);
                res[q] = acc * vv;
            }
            tile[(8 * c8 + j) * 33 + (31 - r)] = pk2(res[1], res[0]);
        }
        __syncthreads();
#pragma unroll
        for (int i = 0; i < 2; ++i) {
            const int idx = t + 256 * i;
            const int d = idx >> 3, q = idx & 7;
            u32x4 o;
#pragma unroll
            for (int c = 0; c < 4; ++c) o[c] = tile[d * 33 + 4 * q + c];
            *reinterpret_cast<u32x4*>(kvR + (size_t)(d0 + d) * S_ + (S_ - 64 - s0) + 8 * q) = o;
        }
    } else if (blockIdx.x >= 256 + PACK_BLOCKS + 86) {
        const int r0 = (blockIdx.x - (256 + PACK_BLOCKS + 86)) * 16;
        const int row = t & 15;
        f32x2* sp = reinterpret_cast<f32x2*>(tile);
        u32x4 a[4];
#pragma unroll
        for (int i = 0; i < 4; ++i) a[i] = *reinterpret_cast<const u32x4*>(x0 + (size_t)(r0 + row) * D_ + 8 * ((t >> 4) + 16 * i));
#pragma unroll
        for (int i = 0; i < 4; ++i) {
            const int o = (t >> 4) + 16 * i;
            u32x2 lo = {a[i][0], a[i][1]}, hi = {a[i][2], a[i][3]};
            x0t[(size_t)(2 * o) * S_ + r0 + row] = lo;
            x0t[(size_t)(2 * o + 1) * S_ + r0 + row] = hi;
            float sm = 0.f, sq = 0.f;
#pragma unroll
            for (int j = 0; j < 4; ++j) {
                const float l0 = bflo(a[i][j]), h0 = bfhi(a[i][j]);
                sm += l0 + h0;
                sq += l0 * l0 + h0 * h0;
            }
            f32x2 e = {sm, sq};
            sp[row * 64 + o] = e;
        }
        __syncthreads();
        if (t < 128) {
            const int rr = t >> 3, g = t & 7;
            f32x2 e = sp[rr * 64 + 8 * g];
#pragma unroll
            for (int j = 1; j < 8; ++j) e += sp[rr * 64 + 8 * g + j];
            part0[(size_t)(r0 + rr) * 8 + g] = e;
        }
    } else if (blockIdx.x >= 256 + PACK_BLOCKS) {
        const int cbk = blockIdx.x - (256 + PACK_BLOCKS);
        const int m = cbk / 43, f0 = (cbk % 43) * 32;
        const u16* src = m ? wu : wg;
        const int np = t & 15, kg = t >> 4;
        unsigned wv[32];
#pragma unroll
        for (int j = 0; j < 32; ++j) wv[j] = *reinterpret_cast<const unsigned*>(src + (size_t)(32 * kg + j) * F_ + f0 + 2 * np);
        float s0 = 0.f, s1 = 0.f;
#pragma unroll
        for (int j = 0; j < 32; ++j) { s0 += bflo(wv[j]); s1 += bfhi(wv[j]); }
        float* red = reinterpret_cast<float*>(tile);
        red[kg * 33 + 2 * np] = s0;
        red[kg * 33 + 2 * np + 1] = s1;
        __syncthreads();
        if (t < 32) {
            float a = 0.f;
#pragma unroll
            for (int j = 0; j < 16; ++j) a += red[j * 33 + t];
            csum[m * F_ + f0 + t] = a;
        }
    } else {
        int id = (blockIdx.x - 256) * 256 + t;
        if (id < PACK_GU_THREADS) {
            const int np = id % 688;
            const int rest = id / 688;
            const int ko = rest & 63, m = rest >> 6;
            const int n = 2 * np;
            const int ft = n >> 4, ks = ko >> 2, g = ko & 3;
            pack_item(m ? wu : wg, F_, ko, np, Wgu + ((size_t)((ft * KS1 + ks) * 2 + m) * 64 + g * 16 + (n & 15)));
        } else {
            id -= PACK_GU_THREADS;
            const int np = id & 255;
            const int ko = id >> 8;
            const int n = 2 * np;
            const int nt = n >> 4, ks = ko >> 2, g = ko & 3;
            pack_item(wd, D_, ko, np, Wd + ((size_t)(nt * KS2 + ks) * 64 + g * 16 + (n & 15)));
        }
    }
}

constexpr int CV_C = 4160;
constexpr int CV_U = 4 * CV_C;
constexpr int CV_CH = CV_U + 4608;
constexpr int CV_Y = 4 * CV_CH;

template <int OFF> __device__ __forceinline__ u32x2 lds8(unsigned addr) {
    u32x2 r;
    asm volatile("ds_read_b64 %0, %1 offset:%2" : "=v"(r) : "v"(addr), "i"(OFF) : "memory");
    return r;
}
template <int OFF> __device__ __forceinline__ u32x4 lds16(unsigned addr) {
    u32x4 r;
    asm volatile("ds_read_b128 %0, %1 offset:%2" : "=v"(r) : "v"(addr), "i"(OFF) : "memory");
    return r;
}
__device__ __forceinline__ void lgkm_wait(u32x2 (&a)[16], u32x4 (&b)[8]) {
    asm volatile("s_waitcnt lgkmcnt(0)"
                 : "+v"(a[0]), "+v"(a[1]), "+v"(a[2]), "+v"(a[3]), "+v"(a[4]), "+v"(a[5]), "+v"(a[6]), "+v"(a[7]),
                   "+v"(a[8]), "+v"(a[9]), "+v"(a[10]), "+v"(a[11]), "+v"(a[12]), "+v"(a[13]), "+v"(a[14]), "+v"(a[15]),
                   "+v"(b[0]), "+v"(b[1]), "+v"(b[2]), "+v"(b[3]), "+v"(b[4]), "+v"(b[5]), "+v"(b[6]), "+v"(b[7])
                 :: "memory");
}
template <int BLK, int GBLK>
__device__ __forceinline__ void conv_load(unsigned fa, unsigned ga, u32x2 (&F)[16], u32x4 (&G)[8]) {
#define CVL(i) F[2 * (i)] = lds8<64 * (8 * BLK + (i))>(fa); F[2 * (i) + 1] = lds8<64 * (8 * BLK + (i)) + 8>(fa); \
               G[i] = lds16<64 * (8 * (GBLK + 7) + (i))>(ga);
    CVL(0) CVL(1) CVL(2) CVL(3) CVL(4) CVL(5) CVL(6) CVL(7)
#undef CVL
}
__device__ __forceinline__ u32x4 cat(u32x2 a, u32x2 b) { u32x4 r = {a[0], a[1], b[0], b[1]}; return r; }

template <int N, int T>
__device__ __forceinline__ void conv_step(unsigned fa, unsigned ga, u32x2 (&Fr)[3][16], u32x4 (&Gr)[2][8], f32x4& acc1,
                                          f32x4& acc0) {
    if (T >= 0) lgkm_wait(Fr[T % 3], Gr[T & 1]);
    if (T + 1 <= N + 1) conv_load<T + 1, T - N>(fa, ga, Fr[(T + 1) % 3], Gr[(T + 1) & 1]);
    __builtin_amdgcn_sched_barrier(0);
    if (T >= 0) {
#pragma unroll
        for (int i = 0; i < 8; ++i) {
            acc1 = mfma16(cat(Fr[T % 3][2 * i], Fr[T % 3][2 * i + 1]), Gr[T & 1][i], acc1);
            if (T >= 1) acc0 = mfma16(cat(Fr[(T + 2) % 3][2 * i], Fr[(T + 2) % 3][2 * i + 1]), Gr[T & 1][i], acc0);
        }
    }
    __builtin_amdgcn_sched_barrier(0);
    if constexpr (T < N + 1) conv_step<N, T + 1>(fa, ga, Fr, Gr, acc1, acc0);
}
template <int N>
__device__ __forceinline__ void conv_task(unsigned fa, unsigned ga, f32x4& acc1, f32x4& acc0) {
    u32x2 Fr[3][16];
    u32x4 Gr[2][8];
    conv_step<N, -1>(fa, ga, Fr, Gr, acc1, acc0);
}

__device__ __forceinline__ unsigned sh16(unsigned lo, unsigned hi) { return (lo >> 16) | (hi << 16); }

__global__ __launch_bounds__(256) void conv_kernel(const u16* __restrict__ kvR, const u16* __restrict__ ssm_k,
                                                   const u16* __restrict__ ssm_D, u16* __restrict__ Y4) {
    __shared__ __attribute__((aligned(16))) char smem[CV_Y + 8704];
    const int t = threadIdx.x, l = t & 63;
    const int w = __builtin_amdgcn_readfirstlane(t >> 6);
    const int cq = blockIdx.x >> 1, half = blockIdx.x & 1;
    const int ch = 4 * cq + w;
    char* base = smem + w * CV_CH;
    const float Dv = bflo((unsigned)ssm_D[ch]);
    {
        const u16* hp = ssm_k + (size_t)ch * S_;
        const u16* up = kvR + (size_t)ch * S_;
        u32x4 hv[4], uv[4];
        u32x2 hx[4];
#pragma unroll
        for (int i = 0; i < 4; ++i) {
            const int m = l + 64 * i;
            hv[i] = *reinterpret_cast<const u32x4*>(hp + 8 * m);
            uv[i] = *reinterpret_cast<const u32x4*>(up + 8 * m);
            const u32x2 zz = {0u, 0u};
            hx[i] = (m < 255) ? *reinterpret_cast<const u32x2*>(hp + 8 * m + 8) : zz;
        }
        const u32x4 z = {0u, 0u, 0u, 0u};
        if (l < 32) *reinterpret_cast<u32x4*>(base + CV_U + 4096 + 16 * l) = z;
        if (l < 4) *reinterpret_cast<u32x4*>(base + l * CV_C) = z;
        if (l == 0) {
            const unsigned h01 = hv[0][0], h23 = hv[0][1];
            u32x4 c0 = z, c1 = z, c2 = z, c3 = z;
            c0[3] = h01 << 16;
            c1[3] = h01;
            c2[2] = h01 << 16; c2[3] = sh16(h01, h23);
            c3[2] = h01;       c3[3] = h23;
            *reinterpret_cast<u32x4*>(base + 0 * CV_C + 16) = c0;
            *reinterpret_cast<u32x4*>(base + 1 * CV_C + 16) = c1;
            *reinterpret_cast<u32x4*>(base + 2 * CV_C + 16) = c2;
            *reinterpret_cast<u32x4*>(base + 3 * CV_C + 16) = c3;
        }
#pragma unroll
        for (int i = 0; i < 4; ++i) {
            const int m = l + 64 * i;
            const unsigned d0 = hv[i][0], d1 = hv[i][1], d2 = hv[i][2], d3 = hv[i][3], d4 = hx[i][0], d5 = hx[i][1];
            const unsigned e0 = sh16(d0, d1), e1 = sh16(d1, d2), e2 = sh16(d2, d3), e3 = sh16(d3, d4), e4 = sh16(d4, d5);
            u32x4 c0 = {e0, e1, e2, e3}, c1 = {d1, d2, d3, d4}, c2 = {e1, e2, e3, e4}, c3 = {d2, d3, d4, d5};
            *reinterpret_cast<u32x4*>(base + 0 * CV_C + 32 + 16 * m) = c0;
            *reinterpret_cast<u32x4*>(base + 1 * CV_C + 32 + 16 * m) = c1;
            *reinterpret_cast<u32x4*>(base + 2 * CV_C + 32 + 16 * m) = c2;
            *reinterpret_cast<u32x4*>(base + 3 * CV_C + 32 + 16 * m) = c3;
            *reinterpret_cast<u32x4*>(base + CV_U + 16 * m) = uv[i];
        }
    }
    __syncthreads();

    const int o0 = (l & 15) + 8 * (l >> 4);
    const unsigned fp = (unsigned)(size_t)(base + (l & 3) * CV_C + 2 * (o0 & ~3));
    const unsigned gp = (unsigned)(size_t)(base + CV_U + 2 * (2032 - 16 * (l & 15) + 8 * (l >> 4)) - 64 * 56);
    f32x4 a0 = {0.f, 0.f, 0.f, 0.f}, a1 = a0, a2 = a0, a3 = a0;
    int n0, n1, n2, n3;
    if (half == 0) {
        n0 = 7; n1 = 6; n2 = 1; n3 = 0;
        conv_task<6>(fp, gp, a0, a1);
        conv_task<0>(fp, gp, a2, a3);
    } else {
        n0 = 5; n1 = 4; n2 = 3; n3 = 2;
        conv_task<4>(fp, gp, a0, a1);
        conv_task<2>(fp, gp, a2, a3);
    }
    u16* yb = reinterpret_cast<u16*>(smem + CV_Y);
    const int sl = 16 * (l & 15) + 4 * (l >> 4);
    const int slp = sl + (l & 15);
#define EPI(acc, n, i)                                                                        \
    {                                                                                         \
        const int s = 256 * (n) + sl;                                                         \
        const u32x2 uu = *reinterpret_cast<const u32x2*>(base + CV_U + 2 * (2044 - s));       \
        const float u0 = bfhi(uu[1]), u1 = bflo(uu[1]), u2 = bfhi(uu[0]), u3 = bflo(uu[0]);   \
        const unsigned c01 = pk2(acc[0], acc[1]), c23 = pk2(acc[2], acc[3]);                  \
        const unsigned k01 = pk2(u0 * Dv, u1 * Dv), k23 = pk2(u2 * Dv, u3 * Dv);              \
        const unsigned p01 = pk2(bflo(c01) + bflo(k01), bfhi(c01) + bfhi(k01));               \
        const unsigned p23 = pk2(bflo(c23) + bflo(k23), bfhi(c23) + bfhi(k23));               \
        u16* q = yb + ((i) * 272 + slp) * 4 + w;                                              \
        q[0] = (u16)p01; q[4] = (u16)(p01 >> 16); q[8] = (u16)p23; q[12] = (u16)(p23 >> 16);  \
    }
    EPI(a0, n0, 0) EPI(a1, n1, 1) EPI(a2, n2, 2) EPI(a3, n3, 3)
    __syncthreads();
    {
        const u32x2* yr = reinterpret_cast<const u32x2*>(smem + CV_Y) + t + (t >> 4);
        u32x2* dst = reinterpret_cast<u32x2*>(Y4) + (size_t)cq * S_;
        dst[256 * n0 + t] = yr[0];
        dst[256 * n1 + t] = yr[272];
        dst[256 * n2 + t] = yr[544];
        dst[256 * n3 + t] = yr[816];
    }
}

__device__ __forceinline__ float rowsum8(float v) {
    v += __builtin_bit_cast(float, __builtin_amdgcn_update_dpp(0, __builtin_bit_cast(int, v), 0x128, 0xf, 0xf, false));
    {
        const unsigned u = __builtin_bit_cast(unsigned, v);
        auto r = __builtin_amdgcn_permlane16_swap(u, u, false, false);
        v = __builtin_bit_cast(float, (unsigned)r[0]) + __builtin_bit_cast(float, (unsigned)r[1]);
    }
    {
        const unsigned u = __builtin_bit_cast(unsigned, v);
        auto r = __builtin_amdgcn_permlane32_swap(u, u, false, false);
        v = __builtin_bit_cast(float, (unsigned)r[0]) + __builtin_bit_cast(float, (unsigned)r[1]);
    }
    return v;
}
__device__ __forceinline__ f32x2 unpk(unsigned u) { f32x2 r = {bflo(u), bfhi(u)}; return r; }

__device__ __forceinline__ void g1_epilogue(const f32x4 (&ag)[4], const f32x4 (&au)[4], int rb, int ft, int l,
                                            u32x2* __restrict__ hpk, const f32x2* st2, u32x4 cgu, u32x4 cuu) {
    const int ks2 = ft >> 1;
    const int slot = (2 * (ft & 1) + (l >> 5)) * 16 + (l & 15);
    const f32x4 cg = __builtin_bit_cast(f32x4, cgu), cu = __builtin_bit_cast(f32x4, cuu);
    float sa[4], sb[4];
#pragma unroll
    for (int rt = 0; rt < 4; ++rt) {
        const f32x4 pp = *reinterpret_cast<const f32x4*>(st2 + 2 * (16 * rt + (l & 15)));
        const float mean2 = (pp[0] + pp[2]) * (1.0f / 512.0f);
        const float inv2 = __builtin_amdgcn_rsqf(fmaxf((pp[1] + pp[3]) * (1.0f / 512.0f) - mean2 * mean2, 0.f) + 1e-5f);
        sa[rt] = inv2;
        sb[rt] = -mean2 * inv2;
    }
    u32x2* dst = hpk + ((size_t)(rb * 4 * KS2 + ks2) * 64 + slot) * 2 + ((l >> 4) & 1);
#pragma unroll
    for (int rt = 0; rt < 4; ++rt) {
        const f32x4 g4 = ag[rt] * sa[rt] + cg * sb[rt];
        const f32x4 u4 = au[rt] * sa[rt] + cu * sb[rt];
        const unsigned g01 = pk2(g4[0], g4[1]), g23 = pk2(g4[2], g4[3]);
        const unsigned u01 = pk2(u4[0], u4[1]), u23 = pk2(u4[2], u4[3]);
        const float g0 = bflo(g01), g1 = bfhi(g01), g2 = bflo(g23), g3 = bfhi(g23);
        const float e0 = __expf(-g0), e1 = __expf(-g1), e2 = __expf(-g2), e3 = __expf(-g3);
        const float r0 = __builtin_amdgcn_rcpf(1.0f + e0), r1 = __builtin_amdgcn_rcpf(1.0f + e1);
        const float r2 = __builtin_amdgcn_rcpf(1.0f + e2), r3 = __builtin_amdgcn_rcpf(1.0f + e3);
        const unsigned s01 = pk2(g0 * r0, g1 * r1), s23 = pk2(g2 * r2, g3 * r3);
        u32x2 o;
        o[0] = pk2(bflo(s01) * bflo(u01), bfhi(s01) * bfhi(u01));
        o[1] = pk2(bflo(s23) * bflo(u23), bfhi(s23) * bfhi(u23));
        dst[(size_t)rt * KS2 * 128] = o;
    }
}

constexpr int G1_WIN = 6;
__device__ constexpr int g1_ks(int t) { return t < 4 ? t : t < 8 ? t + 4 : t < 12 ? t - 4 : t; }
__device__ constexpr int g1_younger(int t) { return 2 * ((t - 1 + G1_WIN < 15 ? t - 1 + G1_WIN : 15) - t); }
#define G1_LOADW(t)                                                                                           \
    {                                                                                                         \
        wf[(t) % G1_WIN][0] = gld16<((2 * g1_ks(t)) & 3) * 1024>(wbase, wo + ((2 * g1_ks(t)) >> 2) * 4096);     \
        wf[(t) % G1_WIN][1] = gld16<((2 * g1_ks(t) + 1) & 3) * 1024>(wbase, wo + ((2 * g1_ks(t)) >> 2) * 4096); \
    }
#define G1_STEP(t)                                                                            \
    {                                                                                         \
        vm_wait2<g1_younger(t)>(wf[(t) % G1_WIN][0], wf[(t) % G1_WIN][1]);                    \
        _Pragma("unroll") for (int rt = 0; rt < 4; ++rt) {                                    \
            const u32x4 bb = At[(rt * KS1 + g1_ks(t)) * 64 + l];                              \
            ag[rt] = mfma16(wf[(t) % G1_WIN][0], bb, ag[rt]);                                 \
            au[rt] = mfma16(wf[(t) % G1_WIN][1], bb, au[rt]);                                 \
        }                                                                                     \
        if ((t) + G1_WIN < 16) G1_LOADW((t) + G1_WIN)                                         \
    }
#define G1_STEPS8(b)                                                                          \
    G1_STEP(b + 0) G1_STEP(b + 1) G1_STEP(b + 2) G1_STEP(b + 3) G1_STEP(b + 4) G1_STEP(b + 5) \
    G1_STEP(b + 6) G1_STEP(b + 7)

template <bool LATE>
__device__ __forceinline__ void g1_mfma_role(const u32x4* At, const f32x2* st2, const u32x4* wbase,
                                             const float* csum, int l, int rb, int ft, u32x2* __restrict__ hpk) {
    const unsigned wo = 16 * l;
    u32x4 wf[G1_WIN][2];
    const unsigned co = (16 * ft + 4 * (l >> 4)) * 4;
    u32x4 cs0 = gld16<0>(csum, co);
    u32x4 cs1 = gld16<0>(csum, co + F_ * 4);
    G1_LOADW(0) G1_LOADW(1) G1_LOADW(2) G1_LOADW(3)
    __syncthreads();
    G1_LOADW(4) G1_LOADW(5)
    f32x4 ag[4], au[4];
#pragma unroll
    for (int r = 0; r < 4; ++r) { ag[r] = (f32x4){0.f, 0.f, 0.f, 0.f}; au[r] = ag[r]; }
    G1_STEPS8(0)
    if (!LATE) __syncthreads();
    G1_STEPS8(8)
    vm_wait2<0>(cs0, cs1);
    g1_epilogue(ag, au, rb, ft, l, hpk, st2, cs0, cs1);
}

__global__ __launch_bounds__(1024, 4) void g1_kernel(const u32x2* __restrict__ x, const f32x2* __restrict__ part,
                                                     const u32x2* __restrict__ Y4, const u32x4* __restrict__ Wgu,
                                                     const float* __restrict__ csum, u32x2* __restrict__ x1out,
                                                     u32x2* __restrict__ hpk) {
    __shared__ u32x4 At[4 * KS1 * 64 + 64];
    f32x2* st2 = reinterpret_cast<f32x2*>(At + 4 * KS1 * 64);
    const int b = blockIdx.x;
    const int rb = (b & 7) * 4 + (b >> 6), cb = (b >> 3) & 7;
    const int l = threadIdx.x & 63;
    const int w = __builtin_amdgcn_readfirstlane(threadIdx.x >> 6);
    const int ntl = cb < 6 ? 11 : 10;
    const int ft0 = cb < 6 ? 11 * cb : 66 + 10 * (cb - 6);
    if (w < 8) {
        const int ip = l & 7, g = l >> 3;
        const int rt = w >> 1, dh = w & 1;
        const int row = rb * 64 + 16 * rt + 2 * ip;
        u32x4 xv[8], yv[8];
        u32x2 pa = gld8<0>(part, (unsigned)(row * 8 + g) * 8);
        u32x2 pb = gld8<64>(part, (unsigned)(row * 8 + g) * 8);
        const unsigned o0 = (unsigned)((64 * dh + g) * S_ + row) * 8;
#pragma unroll
        for (int c = 0; c < 8; ++c) {
            xv[c] = gld16<0>(x, o0 + c * (8 * S_ * 8));
            yv[c] = gld16<0>(Y4, o0 + c * (8 * S_ * 8));
        }
        vm_wait2<16>(pa, pb);
        const float meanA = rowsum8(__builtin_bit_cast(float, (unsigned)pa[0])) * (1.0f / 512.0f);
        const float msqA = rowsum8(__builtin_bit_cast(float, (unsigned)pa[1])) * (1.0f / 512.0f);
        const float invA = 1.0f / sqrtf(fmaxf(msqA - meanA * meanA, 0.f) + 1e-5f);
        const float meanB = rowsum8(__builtin_bit_cast(float, (unsigned)pb[0])) * (1.0f / 512.0f);
        const float msqB = rowsum8(__builtin_bit_cast(float, (unsigned)pb[1])) * (1.0f / 512.0f);
        const float invB = 1.0f / sqrtf(fmaxf(msqB - meanB * meanB, 0.f) + 1e-5f);
        const f32x2 iA = {invA, invA}, bA = {-meanA * invA, -meanA * invA};
        const f32x2 iB = {invB, invB}, bB = {-meanB * invB, -meanB * invB};
        f32x2 sa = {0.f, 0.f}, sb = sa, qa = sa, qb = sa;
        u32x2* At2 = reinterpret_cast<u32x2*>(At);
#define G1_TRANS(c)                                                                                   \
        {                                                                                             \
            vm_wait2<14 - 2 * (c)>(xv[c], yv[c]);                                                     \
            u32x4 o;                                                                                  \
            _Pragma("unroll") for (int j = 0; j < 4; ++j) {                                           \
                const f32x2 xx = unpk(xv[c][j]);                                                      \
                const f32x2 yy = unpk(yv[c][j]);                                                      \
                const f32x2 q = (j < 2) ? (xx * iA + bA) : (xx * iB + bB);                            \
                const f32x2 r = xx + q * yy;                                                          \
                o[j] = pk2(r[0], r[1]);                                                               \
                if (j < 2) { sa += r; qa += r * r; } else { sb += r; qb += r * r; }                   \
            }                                                                                         \
            const int unit = (rt * KS1 + 8 * dh + (c)) * 64 + (g >> 1) * 16 + 2 * ip;                 \
            u32x2 oa = {o[0], o[1]}, ob = {o[2], o[3]};                                               \
            At2[2 * unit + (g & 1)] = oa;                                                             \
            At2[2 * (unit + 1) + (g & 1)] = ob;                                                       \
            xv[c] = o;                                                                                \
        }
        G1_TRANS(0) G1_TRANS(1) G1_TRANS(2) G1_TRANS(3)
        __syncthreads();
        G1_TRANS(4) G1_TRANS(5) G1_TRANS(6) G1_TRANS(7)
#pragma unroll
        for (int c = 0; c < 8; ++c)
            if ((8 * dh + c) >> 1 == cb)
                *reinterpret_cast<u32x4*>(x1out + (size_t)(64 * dh + 8 * c + g) * S_ + row) = xv[c];
        const float s_a = rowsum8(sa[0] + sa[1]), q_a = rowsum8(qa[0] + qa[1]);
        const float s_b = rowsum8(sb[0] + sb[1]), q_b = rowsum8(qb[0] + qb[1]);
        if (g == 0) {
            f32x2 ea = {s_a, q_a}, eb = {s_b, q_b};
            st2[2 * (16 * rt + 2 * ip) + dh] = ea;
            st2[2 * (16 * rt + 2 * ip + 1) + dh] = eb;
        }
        if (w + 8 < ntl) g1_mfma_role<true>(At, st2, Wgu + (size_t)(ft0 + 8 + w) * (KS1 * 2 * 64), csum, l, rb, ft0 + 8 + w, hpk);
        else __syncthreads();
    } else {
        g1_mfma_role<false>(At, st2, Wgu + (size_t)(ft0 + w - 8) * (KS1 * 2 * 64), csum, l, rb, ft0 + w - 8, hpk);
    }
}

__device__ __forceinline__ void g2_main(const u32x4* hbase, const u32x4* wbase, int l, bool six, f32x4 (&acc)[4][4]) {
    u32x4 af[5][4], bf[5][4];
    unsigned vo[4];
#pragma unroll
    for (int i = 0; i < 4; ++i) vo[i] = 16 * l + i * (KS2 * 1024);
#define G2_LOAD(st)                                                                                   \
    _Pragma("unroll") for (int i = 0; i < 4; ++i) {                                                   \
        af[(st) % 5][i] = gld16<((st) & 3) * 1024>(wbase, vo[i] + ((st) >> 2) * 4096);                \
        bf[(st) % 5][i] = gld16<((st) & 3) * 1024>(hbase, vo[i] + ((st) >> 2) * 4096);                \
    }
#define G2_STEP(st, nwait)                                                                            \
    {                                                                                                 \
        vm_wait8<nwait>(af[(st) % 5], bf[(st) % 5]);                                                  \
        _Pragma("unroll") for (int i = 0; i < 4; ++i)                                                 \
            _Pragma("unroll") for (int j = 0; j < 4; ++j)                                             \
                acc[i][j] = mfma16(af[(st) % 5][i], bf[(st) % 5][j], acc[i][j]);                      \
        __builtin_amdgcn_sched_barrier(0);                                                            \
    }
    G2_LOAD(0) G2_LOAD(1) G2_LOAD(2)
    G2_STEP(0, 16) G2_LOAD(3)
    G2_STEP(1, 16) G2_LOAD(4)
    G2_STEP(2, 16)
    if (six) { G2_LOAD(5) }
    G2_STEP(3, 8) G2_STEP(4, 0)
    if (six) { G2_STEP(5, 0) }
}

__device__ __forceinline__ float quadsum(float v) {
    {
        const unsigned u = __builtin_bit_cast(unsigned, v);
        auto r = __builtin_amdgcn_permlane16_swap(u, u, false, false);
        v = __builtin_bit_cast(float, (unsigned)r[0]) + __builtin_bit_cast(float, (unsigned)r[1]);
    }
    {
        const unsigned u = __builtin_bit_cast(unsigned, v);
        auto r = __builtin_amdgcn_permlane32_swap(u, u, false, false);
        v = __builtin_bit_cast(float, (unsigned)r[0]) + __builtin_bit_cast(float, (unsigned)r[1]);
    }
    return v;
}

__global__ __launch_bounds__(512, 2) void g2_kernel(const u32x4* __restrict__ hpk, const u32x4* __restrict__ Wd,
                                                    const u32x2* __restrict__ xin, u32x2* __restrict__ xout,
                                                    f32x2* __restrict__ part, int STATS) {
    __shared__ f32x4 red[8 * 16 * 64 + 128];
    f32x2* sred = reinterpret_cast<f32x2*>(red + 8 * 16 * 64);
    const int b = blockIdx.x;
    const int rb = (b & 7) * 4 + (b >> 6), cb = (b >> 3) & 7;
    const int l = threadIdx.x & 63;
    const int w = __builtin_amdgcn_readfirstlane(threadIdx.x >> 6);
    const int start = 5 * w + (w < 3 ? w : 3);
    const u32x4* hbase = hpk + ((size_t)(rb * 4) * KS2 + start) * 64;
    const u32x4* wbase = Wd + ((size_t)(cb * 4) * KS2 + start) * 64;
    const int n = cb * 64 + 16 * (w >> 1) + 4 * (l >> 4);
    const int s0 = rb * 64 + 32 * (w & 1) + (l & 15);
    const unsigned xo = (unsigned)((n >> 2) * S_ + s0) * 8;
    u32x2 xr0 = gld8<0>(xin, xo), xr1 = gld8<128>(xin, xo);
    f32x4 acc[4][4];
#pragma unroll
    for (int i = 0; i < 4; ++i)
#pragma unroll
        for (int j = 0; j < 4; ++j) acc[i][j] = (f32x4){0.f, 0.f, 0.f, 0.f};
    g2_main(hbase, wbase, l, w < 3, acc);
#pragma unroll
    for (int i = 0; i < 4; ++i)
#pragma unroll
        for (int j = 0; j < 4; ++j) red[(w * 16 + i * 4 + j) * 64 + l] = acc[i][j];
    __syncthreads();
    vm_wait2<0>(xr0, xr1);
#pragma unroll
    for (int q = 0; q < 2; ++q) {
        const int tile = 2 * w + q;
        const u32x2 xr = q ? xr1 : xr0;
        f32x4 sum = red[tile * 64 + l];
#pragma unroll
        for (int p = 1; p < 8; ++p) sum += red[(p * 16 + tile) * 64 + l];
        u32x2 o;
        o[0] = pk2(bflo(xr[0]) + rbf(sum[0]), bfhi(xr[0]) + rbf(sum[1]));
        o[1] = pk2(bflo(xr[1]) + rbf(sum[2]), bfhi(xr[1]) + rbf(sum[3]));
        if (STATS) xout[(size_t)(n >> 2) * S_ + s0 + 16 * q] = o;
        else xout[((size_t)(s0 + 16 * q) * D_ + n) >> 2] = o;
        if (STATS) {
            const float v0 = bflo(o[0]), v1 = bfhi(o[0]), v2 = bflo(o[1]), v3 = bfhi(o[1]);
            const float sm = quadsum((v0 + v1) + (v2 + v3));
            const float sq = quadsum((v0 * v0 + v1 * v1) + (v2 * v2 + v3 * v3));
            if (l < 16) { f32x2 e = {sm, sq}; sred[(w >> 1) * 64 + 32 * (w & 1) + 16 * q + l] = e; }
        }
    }
    if (STATS) {
        __syncthreads();
        if (threadIdx.x < 64) {
            const f32x2 e = (sred[threadIdx.x] + sred[64 + threadIdx.x]) + (sred[128 + threadIdx.x] + sred[192 + threadIdx.x]);
            part[(size_t)(rb * 64 + threadIdx.x) * 8 + cb] = e;
        }
    }
}

extern "C" void kernel_launch(void* const* d_in, const int* in_sizes, int n_in,
                              void* d_out, int out_size, void* d_ws, size_t ws_size,
                              hipStream_t stream) {
    const u16* q       = (const u16*)d_in[0];
    const u16* k       = (const u16*)d_in[1];
    const u16* v       = (const u16*)d_in[2];
    const u16* shift_k = (const u16*)d_in[3];
    const u16* ssm_k   = (const u16*)d_in[4];
    const u16* ssm_D   = (const u16*)d_in[5];
    const u16* w_gate  = (const u16*)d_in[7];
    const u16* w_up    = (const u16*)d_in[8];
    const u16* w_down  = (const u16*)d_in[9];
    u16* out = (u16*)d_out;

    char* ws = (char*)d_ws;
    const size_t MB2 = (size_t)S_ * D_ * 2;
    u16*   kvR = (u16*)(ws);
    u16*   Y4  = (u16*)(ws + MB2);
    u16*   x1  = (u16*)(ws + 2 * MB2);
    u16*   x2  = (u16*)(ws + 3 * MB2);
    u16*   x3  = (u16*)(ws + 4 * MB2);
    u32x4* Wgu = (u32x4*)(ws + 5 * MB2);
    u32x4* Wd  = (u32x4*)(ws + 5 * MB2 + (size_t)NFT * KS1 * 2 * 1024);
    u32x4* hpk = (u32x4*)(ws + 5 * MB2 + (size_t)NFT * KS1 * 2 * 1024 + (size_t)32 * KS2 * 1024);

    char* tail = (char*)hpk + (size_t)128 * KS2 * 1024;
    float* csum  = (float*)tail;
    f32x2* part0 = (f32x2*)(tail + 16384);
    f32x2* part2 = (f32x2*)(tail + 16384 + 131072);

    u32x2* x0t = (u32x2*)(tail + 16384 + 2 * 131072);

    prep_kernel<<<256 + PACK_BLOCKS + 86 + 128, 256, 0, stream>>>(k, v, shift_k, w_gate, w_up, w_down, q, kvR, Wgu, Wd,
                                                                  csum, part0, x0t);
    conv_kernel<<<256, 256, 0, stream>>>(kvR, ssm_k, ssm_D, Y4);
    g1_kernel<<<256, 1024, 0, stream>>>(x0t, part0, (const u32x2*)Y4, Wgu, csum, (u32x2*)x1, (u32x2*)hpk);
    g2_kernel<<<256, 512, 0, stream>>>(hpk, Wd, (const u32x2*)x1, (u32x2*)x2, part2, 1);
    g1_kernel<<<256, 1024, 0, stream>>>((const u32x2*)x2, part2, (const u32x2*)Y4, Wgu, csum, (u32x2*)x3, (u32x2*)hpk);
    g2_kernel<<<256, 512, 0, stream>>>(hpk, Wd, (const u32x2*)x3, (u32x2*)out, part2, 0);
}
```
